```python
import torch

def _softmax(x, axis=-1):
    return torch.softmax(x, dim=axis)

def _gelu(x):
    return torch.nn.functional.gelu(x, approximate='tanh')

def _silu(x):
    return torch.nn.functional.silu(x)

def _sigmoid(x):
    return torch.sigmoid(x)

def _layernorm(x, axis=-1, eps=1e-05):
    m = x.mean(dim=axis, keepdim=True)
    v = x.var(dim=axis, keepdim=True, unbiased=False)
    return (x - m) / torch.sqrt(v + eps)

def _scatter_add(tgt, idx, src, axis=0):
    return tgt.clone().index_add_(axis, idx, src)

def _segment_sum(src, seg, n):
    out = torch.zeros((n,) + src.shape[1:], dtype=src.dtype)
    return out.index_add_(0, seg, src)

def _pad(x, pw):
    flat = tuple((v for pair in reversed(pw) for v in pair))
    return torch.nn.functional.pad(x, flat)
kernel_name = 'adaLN_cond__spectral_gfnet__glu_gated_bilinear__prompt'

def setup_inputs():

    _w = lambda a: (lambda t: t.to(torch.bfloat16) if t.is_floating_point() else t)(torch.as_tensor(a))

    def _A_setup():
        B, S, D = (1, 2048, 512)
        Df = D // 2 + 1
        return {'x': _w((lambda *s: torch.randn(*s, dtype=torch.bfloat16) * 0.05)(B, S, D)), 'w_r': _w((lambda *s: torch.randn(*s, dtype=torch.bfloat16) * 3.2)(S, Df) * 0.02), 'w_i': _w((lambda *s: torch.randn(*s, dtype=torch.bfloat16) * 3.2)(S, Df) * 0.02)}
    _A_in = _A_setup()

    def _B_setup():
        B, S, D, F = (1, 2048, 512, 1376)
        return {'x': _w((lambda *s: torch.randn(*s, dtype=torch.bfloat16) * 0.05)(B, S, D)), 'w_gate': _w((lambda *s: torch.randn(*s, dtype=torch.bfloat16) * 0.4)(D, F) * D ** (-0.5)), 'w_up': _w((lambda *s: torch.randn(*s, dtype=torch.bfloat16) * 0.4)(D, F) * D ** (-0.5)), 'w_down': _w((lambda *s: torch.randn(*s, dtype=torch.bfloat16) * 0.4)(F, D) * F ** (-0.5))}
    _B_in = _B_setup()
    return {'A_x': _A_in['x'], 'A_w_r': _A_in['w_r'], 'A_w_i': _A_in['w_i'], 'B_x': _B_in['x'], 'B_w_gate': _B_in['w_gate'], 'B_w_up': _B_in['w_up'], 'B_w_down': _B_in['w_down'], 'c': _w((lambda *s: torch.randn(*s, dtype=torch.bfloat16) * 0.1)(1, 512) * 8.0), 'w_mod': _w((lambda *s: torch.randn(*s, dtype=torch.bfloat16) * 0.1)(512, 6 * 512) * 512 ** (-0.5) * 8.0)}

def reference(A_x, A_w_r, A_w_i, B_x, B_w_gate, B_w_up, B_w_down, c, w_mod):

    def _A_ref(x, w_r, w_i):
        B, S, D = x.shape
        X = (lambda x, axes=(-2, -1): torch.fft.rfft2(x.float(), dim=axes))(x, axes=(-2, -1))
        W = w_r + 1j * w_i
        X = X * W
        y = (lambda x, s, axes=(-2, -1): torch.fft.irfft2(x, s=s, dim=axes).to(torch.bfloat16))(X, (S, D), axes=(-2, -1))
        return y

    def _B_ref(x, w_gate, w_up, w_down):
        g = torch.einsum('bsd,df->bsf', x, w_gate)
        u = torch.einsum('bsd,df->bsf', x, w_up)
        h = g * u
        return torch.einsum('bsf,fd->bsd', h, w_down)
    x = A_x
    mod = (_silu(c) @ w_mod).reshape(c.shape[0], 6, 1, -1)
    sa, ca, ga, sb, cb, gb = (mod[:, 0].clone(), mod[:, 1].clone(), mod[:, 2].clone(), mod[:, 3].clone(), mod[:, 4].clone(), mod[:, 5].clone())
    h = _layernorm(x, axis=-1) * (1 + ca) + sa
    x = x + ga * _A_ref(h, A_w_r, A_w_i)
    h = _layernorm(x, axis=-1) * (1 + cb) + sb
    x = x + gb * _B_ref(h, B_w_gate, B_w_up, B_w_down)
    return x
```

```cpp
#include <hip/hip_runtime.h>
#include <hip/hip_bf16.h>
#include <stdint.h>

typedef __attribute__((ext_vector_type(8))) __bf16 bf16x8;
typedef __attribute__((ext_vector_type(2))) __bf16 bf16x2;
typedef __attribute__((ext_vector_type(4))) float f32x4;
typedef __attribute__((ext_vector_type(2))) float f32x2;
typedef __attribute__((ext_vector_type(4))) unsigned u32x4;
typedef __attribute__((ext_vector_type(2))) unsigned u32x2;
typedef unsigned short u16;

#define DI __device__ __forceinline__

constexpr int S_ = 2048, D_ = 512, F_ = 1376, DF = 257;

constexpr size_t OFF_MOD = 0;
constexpr size_t OFF_WT  = 8192;
constexpr size_t OFF_WGU = OFF_WT + 257u * 2048u * 4u;
constexpr size_t OFF_WD  = OFF_WGU + 43u * 16u * 4u * 1024u;
constexpr size_t OFF_T1  = OFF_WD + 8u * 43u * 4u * 1024u;
constexpr size_t OFF_T2  = OFF_T1 + (2u << 20);
constexpr size_t OFF_H2  = OFF_T2 + (2u << 20);
constexpr size_t OFF_X1  = OFF_H2 + (2u << 20);
constexpr size_t OFF_GU  = OFF_X1 + (2u << 20);

DI unsigned pk(float a, float b) {
  f32x2 v = {a, b};
  return __builtin_bit_cast(unsigned, __builtin_convertvector(v, bf16x2));
}
DI float lo(unsigned u) { return __uint_as_float(u << 16); }
DI float hi(unsigned u) { return __uint_as_float(u & 0xffff0000u); }
DI float rb(float a) { return lo(pk(a, a)); }
DI f32x4 mfma(u32x4 a, u32x4 b, f32x4 c) {
  return __builtin_amdgcn_mfma_f32_16x16x32_bf16(__builtin_bit_cast(bf16x8, a), __builtin_bit_cast(bf16x8, b), c, 0, 0, 0);
}
DI float cosr(float rev) { return __builtin_amdgcn_cosf(rev); }
DI float sinr(float rev) { return __builtin_amdgcn_sinf(rev); }

template <int CTRL> DI float dpp_add(float v) {
  int s = __builtin_amdgcn_update_dpp(0, __float_as_int(v), CTRL, 0xf, 0xf, true);
  return v + __int_as_float(s);
}
DI float wave_sum(float v) {
  v = dpp_add<0xB1>(v); v = dpp_add<0x4E>(v); v = dpp_add<0x141>(v); v = dpp_add<0x140>(v);
  int i = __float_as_int(v);
  float a = __int_as_float(__builtin_amdgcn_readlane(i, 0)), b = __int_as_float(__builtin_amdgcn_readlane(i, 16));
  float c = __int_as_float(__builtin_amdgcn_readlane(i, 32)), d = __int_as_float(__builtin_amdgcn_readlane(i, 48));
  return (a + b) + (c + d);
}

DI void ln_stats(const float (&x)[8], float& mean_b, float& inv_sd) {
  float s = 0.f;
#pragma unroll
  for (int i = 0; i < 8; ++i) s += x[i];
  float mean = wave_sum(s) * (1.f / 512.f);
  float q = 0.f;
#pragma unroll
  for (int i = 0; i < 8; ++i) { float d = x[i] - mean; q += d * d; }
  float var = rb(wave_sum(q) * (1.f / 512.f));
  float sd = rb(__builtin_sqrtf(rb(var + 1e-5f)));
  mean_b = rb(mean);
  inv_sd = 1.0f / sd;
}

DI void transpose8x8(const u32x4 (&r)[8], u32x4 (&o)[8]) {
#pragma unroll
  for (int e = 0; e < 8; ++e)
#pragma unroll
    for (int m = 0; m < 4; ++m)
      o[e][m] = __builtin_amdgcn_perm(r[2 * m + 1][e >> 1], r[2 * m][e >> 1], (e & 1) ? 0x07060302u : 0x05040100u);
}

__global__ __launch_bounds__(256) void k0_prep(const u16* __restrict__ cvec, const u16* __restrict__ wmod,
                                               const u16* __restrict__ wr, const u16* __restrict__ wi,
                                               const u16* __restrict__ wg, const u16* __restrict__ wu,
                                               const u16* __restrict__ wd, char* __restrict__ ws) {
  __shared__ __attribute__((aligned(16))) char sm[2 * 16448];
  const int b = blockIdx.x, tid = threadIdx.x;
  if (b < 96) {
    const int cl = tid & 3, kg = tid >> 2;
    u32x4 w[8];
#pragma unroll
    for (int i = 0; i < 8; ++i) w[i] = *(const u32x4*)(wmod + (size_t)(8 * kg + i) * 3072 + 32 * b + 8 * cl);
    const u32x4 cv = *(const u32x4*)(cvec + 8 * kg);
    float acc[8];
#pragma unroll
    for (int e = 0; e < 8; ++e) acc[e] = 0.f;
#pragma unroll
    for (int i = 0; i < 8; ++i) {
      float c = (i & 1) ? hi(cv[i >> 1]) : lo(cv[i >> 1]);
      float sc = rb(c / (1.f + __expf(-c)));
#pragma unroll
      for (int m = 0; m < 4; ++m) { acc[2 * m] += sc * lo(w[i][m]); acc[2 * m + 1] += sc * hi(w[i][m]); }
    }
    float* red = (float*)sm;
#pragma unroll
    for (int e = 0; e < 8; ++e) { acc[e] = dpp_add<0x124>(acc[e]); acc[e] = dpp_add<0x128>(acc[e]); }
    if ((tid & 15) < 4) {
#pragma unroll
      for (int e = 0; e < 8; ++e) red[(tid >> 4) * 32 + 8 * cl + e] = acc[e];
    }
    __syncthreads();
    if (tid < 32) {
      float s = 0.f;
#pragma unroll
      for (int i = 0; i < 16; ++i) s += red[i * 32 + tid];
      ((u16*)(ws + OFF_MOD))[32 * b + tid] = (u16)(pk(s, s) & 0xffffu);
    }
  } else if (b < 160) {
    const int j0 = 32 * (b - 96);
    const u32x4* sr = (const u32x4*)(wr + (size_t)j0 * DF);
    const u32x4* si = (const u32x4*)(wi + (size_t)j0 * DF);
    for (int i = tid; i < 1028; i += 256) {
      ((u32x4*)sm)[i] = sr[i];
      ((u32x4*)(sm + 16448))[i] = si[i];
    }
    __syncthreads();
    const u16* lr = (const u16*)sm;
    const u16* li = (const u16*)(sm + 16448);
    for (int idx = tid; idx < DF * 8; idx += 256) {
      const int P = idx >> 3, q = idx & 7;
      u32x4 o;
#pragma unroll
      for (int e = 0; e < 4; ++e) o[e] = (unsigned)lr[(4 * q + e) * DF + P] | ((unsigned)li[(4 * q + e) * DF + P] << 16);
      *(u32x4*)(ws + OFF_WT + ((size_t)P * 2048 + j0 + 4 * q) * 4) = o;
    }
  } else if (b < 246) {
    int idx = (b - 160) * 256 + tid;
    const int mat = idx >= 11008;
    idx -= mat * 11008;
    const int ko = idx / 172, no = idx - ko * 172;
    const u16* src = (mat ? wu : wg) + (size_t)(8 * ko) * F_ + 8 * no;
    u32x4 r[8], o[8];
#pragma unroll
    for (int i = 0; i < 8; ++i) r[i] = *(const u32x4*)(src + (size_t)i * F_);
    transpose8x8(r, o);
    const int wc = no >> 2, fr = mat * 2 + ((no >> 1) & 1), ks = ko >> 2, g = ko & 3;
    char* dst = ws + OFF_WGU + (size_t)((wc * 16 + ks) * 4 + fr) * 1024 + (g * 16 + 8 * (no & 1)) * 16;
#pragma unroll
    for (int e = 0; e < 8; ++e) *(u32x4*)(dst + 16 * e) = o[e];
  } else {
    const int idx = (b - 246) * 256 + tid;
    const int dox = idx & 63, g = (idx >> 6) & 3, ks = idx >> 8;
    u32x4 r[8], o[8];
#pragma unroll
    for (int i = 0; i < 8; ++i) {
      const int f = 32 * ks + 4 * g + (i & 3) + 16 * (i >> 2);
      r[i] = *(const u32x4*)(wd + (size_t)f * D_ + 8 * dox);
    }
    transpose8x8(r, o);
    const int dg = dox >> 3, tp = (dox >> 2) & 1, rho = dox & 3;
    char* dst = ws + OFF_WD + (size_t)((dg * 43 + ks) * 4 + 2 * tp) * 1024 + (g * 16 + 4 * rho) * 16;
#pragma unroll
    for (int e = 0; e < 8; ++e) *(u32x4*)(dst + (e >> 2) * 1024 + 16 * (e & 3)) = o[e];
  }
}

__global__ __launch_bounds__(512) void k1_rowfft(const u16* __restrict__ x, char* __restrict__ ws) {
  __shared__ unsigned img[16 * 258];
  const int cb = blockIdx.x, tid = threadIdx.x, w = tid >> 6, l = tid & 63, r = l & 15, g = l >> 4;
  const int q = w & 3, bit = w >> 2;
  const int fI = cb >> 4, t_ = fI >> 2, ks = fI & 3, g_ = (cb & 15) >> 2, rq = cb & 3;
  const int sp = 32 * (8 * ks + 2 * g_ + bit) + 16 * t_ + 4 * rq + q;
  const unsigned* xa = (const unsigned*)(x + (size_t)sp * D_);
  const unsigned* xb = (const unsigned*)(x + (size_t)(sp + 1024) * D_);
  const unsigned* mod = (const unsigned*)(ws + OFF_MOD);
  unsigned ua[4], ub[4], us[4], uc[4];
#pragma unroll
  for (int e = 0; e < 4; ++e) {
    const int dw = 16 * (4 * g + e) + r;
    ua[e] = xa[dw]; ub[e] = xb[dw]; us[e] = mod[dw]; uc[e] = mod[256 + dw];
  }
  u32x4 c1[2], c2[2][2][2];
  float twc[2][4], tws[2][4];
#pragma unroll
  for (int e = 0; e < 4; ++e) {
    const float a = (float)(((4 * g + e) * r) & 15) * (1.f / 16.f);
    const float c = cosr(a), s = sinr(a);
    c1[0][e] = pk(c, s); c1[1][e] = pk(-s, c);
  }
#pragma unroll
  for (int tp = 0; tp < 2; ++tp)
#pragma unroll
    for (int t = 0; t < 2; ++t)
#pragma unroll
      for (int e = 0; e < 4; ++e) {
        const float a = (float)(((2 * (4 * g + e) + t) * (16 * tp + r)) & 31) * (1.f / 32.f);
        const float c = cosr(a), s = sinr(a);
        c2[tp][0][t][e] = pk(c, s); c2[tp][1][t][e] = pk(-s, c);
      }
#pragma unroll
  for (int t = 0; t < 2; ++t)
#pragma unroll
    for (int e = 0; e < 4; ++e) {
      const float a = (float)((2 * (4 * g + e) + t) * r) * (1.f / 512.f);
      twc[t][e] = cosr(a); tws[t][e] = sinr(a);
    }

  float va[8], vb[8];
#pragma unroll
  for (int e = 0; e < 4; ++e) { va[2 * e] = lo(ua[e]); va[2 * e + 1] = hi(ua[e]); vb[2 * e] = lo(ub[e]); vb[2 * e + 1] = hi(ub[e]); }
  float ma, ia, mb, ib;
  ln_stats(va, ma, ia);
  ln_stats(vb, mb, ib);
  u32x4 af[2];
#pragma unroll
  for (int e = 0; e < 4; ++e)
#pragma unroll
    for (int t = 0; t < 2; ++t) {
      const float ca1 = rb(1.f + (t ? hi(uc[e]) : lo(uc[e]))), sa = t ? hi(us[e]) : lo(us[e]);
      unsigned d = pk(va[2 * e + t] - ma, vb[2 * e + t] - mb);
      unsigned n = pk(lo(d) * ia, hi(d) * ib);
      unsigned m = pk(lo(n) * ca1, hi(n) * ca1);
      af[t][e] = pk(lo(m) + sa, hi(m) + sa);
    }
  const f32x4 z = {0.f, 0.f, 0.f, 0.f};
  u32x4 p1[2];
#pragma unroll
  for (int t = 0; t < 2; ++t) {
    const f32x4 re = mfma(af[t], c1[0], z), im = mfma(af[t], c1[1], z);
#pragma unroll
    for (int e = 0; e < 4; ++e)
      p1[t][e] = pk(re[e] * twc[t][e] + im[e] * tws[t][e], im[e] * twc[t][e] - re[e] * tws[t][e]);
  }
#pragma unroll
  for (int tp = 0; tp < 2; ++tp) {
    f32x4 re = mfma(c2[tp][0][0], p1[0], z), im = mfma(c2[tp][1][0], p1[0], z);
    re = mfma(c2[tp][0][1], p1[1], re); im = mfma(c2[tp][1][1], p1[1], im);
#pragma unroll
    for (int e = 0; e < 4; ++e) {
      const int kk = r + 64 * g + 16 * e;
      const int p = tp ? ((256 - kk) & 255) : kk;
      img[(4 * q + 2 * tp + bit) * 258 + p] = pk(re[e], im[e]);
    }
  }
  __syncthreads();
#pragma unroll
  for (int rep = 0; rep < 2; ++rep) {
    const int p = (tid >> 2) + 128 * rep, qq = tid & 3;
    u32x4 o;
#pragma unroll
    for (int e = 0; e < 4; ++e) o[e] = img[(4 * qq + e) * 258 + p];
    *(u32x4*)(ws + OFF_T1 + (size_t)p * 8192 + cb * 64 + qq * 16) = o;
  }
}

__global__ __launch_bounds__(256) void k2_colfft(char* __restrict__ ws) {
  __shared__ __attribute__((aligned(16))) u32x4 ex[8 * 64];
  const int P = blockIdx.x, p = P & 255, tid = threadIdx.x, w = tid >> 6, l = tid & 63, c = l & 15, g = l >> 4;
  const bool special = (p == 0);
  const u32x4* src = (const u32x4*)(ws + OFF_T1 + (size_t)p * 8192);
  u32x4 df[2][4];
#pragma unroll
  for (int t = 0; t < 2; ++t)
#pragma unroll
    for (int ks = 0; ks < 4; ++ks) df[t][ks] = src[(t * 4 + ks) * 64 + l];
  const unsigned* wt = (const unsigned*)(ws + OFF_WT) + (size_t)P * 2048 + 16 * w + c + 256 * g;
  unsigned wv[2][4];
#pragma unroll
  for (int tp = 0; tp < 2; ++tp)
#pragma unroll
    for (int e = 0; e < 4; ++e) wv[tp][e] = wt[64 * (16 * tp + e)];

  const int j1c = 16 * w + c;
  const float sg = (c & 1) ? -1.f : 1.f;
  u32x4 c1[4][2];
#pragma unroll
  for (int ks = 0; ks < 4; ++ks)
#pragma unroll
    for (int bit = 0; bit < 2; ++bit) {
      const float a = (float)(((8 * ks + 2 * g + bit) * j1c) & 63) * (1.f / 64.f);
      const float gr = cosr(a), gi = -sinr(a);
      const float A = 0.5f * (gr + sg * gi), B = 0.5f * (gr - sg * gi);
      unsigned r0 = pk(A, sg * B), r1 = pk(B, sg * A), i0 = pk(-sg * B, A), i1 = pk(sg * A, -B);
      if (special) {
        const unsigned sr = pk(gr, sg * gr), si = pk(gi, sg * gi);
        r0 = P == 0 ? sr : 0u; i0 = P == 0 ? si : 0u; r1 = P == 0 ? 0u : sr; i1 = P == 0 ? 0u : si;
      }
      c1[ks][0][bit] = r0; c1[ks][0][2 + bit] = r1; c1[ks][1][bit] = i0; c1[ks][1][2 + bit] = i1;
    }
  u32x4 c2[2][2][2];
#pragma unroll
  for (int tp = 0; tp < 2; ++tp)
#pragma unroll
    for (int t = 0; t < 2; ++t)
#pragma unroll
      for (int e = 0; e < 4; ++e) {
        const float a = (float)(((16 * t + 4 * g + e) * (16 * tp + c)) & 31) * (1.f / 32.f);
        const float cs = cosr(a), sn = sinr(a);
        c2[tp][0][t][e] = pk(cs, sn); c2[tp][1][t][e] = pk(-sn, cs);
      }
  float t1c[2][4], t1s[2][4], t3c[2][4], t3s[2][4];
#pragma unroll
  for (int t = 0; t < 2; ++t)
#pragma unroll
    for (int e = 0; e < 4; ++e) {
      const float a = (float)((16 * t + 4 * g + e) * j1c) * (1.f / 2048.f);
      t1c[t][e] = cosr(a); t1s[t][e] = sinr(a);
      const float b = (float)((16 * t + c) * (16 * w + 4 * g + e)) * (1.f / 2048.f);
      t3c[t][e] = cosr(b); t3s[t][e] = sinr(b);
    }
  u32x4 c4[4][2];
  const int n1p = 8 * w + (c & 7);
  const bool col1 = c >= 8;
#pragma unroll
  for (int ks = 0; ks < 4; ++ks)
#pragma unroll
    for (int e = 0; e < 4; ++e) {
      const float a = (float)(((16 * ks + 4 * g + e) * n1p) & 63) * (1.f / 64.f);
      const float gr = cosr(a), gi = -sinr(a);
      const float s = (e & 1) ? -1.f : 1.f;
      const float A2 = gr + s * gi, B2 = gr - s * gi;
      unsigned re = col1 ? pk(B2, -s * A2) : pk(A2, s * B2);
      unsigned im = col1 ? pk(s * A2, B2) : pk(s * B2, -A2);
      if (special) { re = pk(gr, -gi); im = pk(s * gr, -s * gi); }
      c4[ks][0][e] = re; c4[ks][1][e] = im;
    }

  const f32x4 z = {0.f, 0.f, 0.f, 0.f};
  u32x4 p1[2], p2[2];
#pragma unroll
  for (int t = 0; t < 2; ++t) {
    f32x4 re = z, im = z;
#pragma unroll
    for (int ks = 0; ks < 4; ++ks) { re = mfma(df[t][ks], c1[ks][0], re); im = mfma(df[t][ks], c1[ks][1], im); }
#pragma unroll
    for (int e = 0; e < 4; ++e)
      p1[t][e] = pk(re[e] * t1c[t][e] + im[e] * t1s[t][e], im[e] * t1c[t][e] - re[e] * t1s[t][e]);
  }
  const float nrm = 1.f / (2048.f * 512.f);
#pragma unroll
  for (int tp = 0; tp < 2; ++tp) {
    f32x4 re = mfma(c2[tp][0][0], p1[0], z), im = mfma(c2[tp][1][0], p1[0], z);
    re = mfma(c2[tp][0][1], p1[1], re); im = mfma(c2[tp][1][1], p1[1], im);
#pragma unroll
    for (int e = 0; e < 4; ++e) {
      const float a = lo(wv[tp][e]) * nrm, b = hi(wv[tp][e]) * nrm;
      p2[tp][e] = pk(re[e] * a - im[e] * b, -(re[e] * b + im[e] * a));
    }
  }
#pragma unroll
  for (int t = 0; t < 2; ++t) {
    f32x4 re = mfma(p2[0], c2[t][0][0], z), im = mfma(p2[0], c2[t][1][0], z);
    re = mfma(p2[1], c2[t][0][1], re); im = mfma(p2[1], c2[t][1][1], im);
    u32x4 p3;
#pragma unroll
    for (int e = 0; e < 4; ++e)
      p3[e] = pk(re[e] * t3c[t][e] + im[e] * t3s[t][e], im[e] * t3c[t][e] - re[e] * t3s[t][e]);
    ex[(w * 2 + t) * 64 + l] = p3;
  }
  __syncthreads();
  const bool do_store = !special || ((g >> 1) == (P >> 8));
#pragma unroll
  for (int t = 0; t < 2; ++t) {
    f32x4 re = z, im = z;
#pragma unroll
    for (int ks = 0; ks < 4; ++ks) {
      const u32x4 b = ex[(ks * 2 + t) * 64 + l];
      re = mfma(c4[ks][0], b, re); im = mfma(c4[ks][1], b, im);
    }
    u32x4 o;
#pragma unroll
    for (int e = 0; e < 4; ++e) o[e] = pk(re[e], im[e]);
    if (do_store) *(u32x4*)(ws + OFF_T2 + (size_t)(w * 32 + 16 * t + c) * 16384 + p * 64 + g * 16) = o;
  }
}

__global__ __launch_bounds__(512) void k3_rowifft(const u16* __restrict__ x, char* __restrict__ ws) {
  constexpr int RS = 580;
  __shared__ unsigned img[8 * RS];
  const int c3 = blockIdx.x, tid = threadIdx.x, w = tid >> 6, l = tid & 63, c = l & 15, g = l >> 4;
  const u32x4* src = (const u32x4*)(ws + OFF_T2 + (size_t)c3 * 16384);
  const u32x4 in0 = src[tid], in1 = src[tid + 512];
  const int sp = 32 * (8 * (c3 >> 5) + w) + (c3 & 31);
  const u16* mod = (const u16*)(ws + OFF_MOD);
  u32x2 xv[2][2], gav[2], sbv[2], cbv[2];
#pragma unroll
  for (int tp = 0; tp < 2; ++tp) {
    const int d0 = 256 * tp + 16 * c + 4 * g;
    xv[0][tp] = *(const u32x2*)(x + (size_t)sp * D_ + d0);
    xv[1][tp] = *(const u32x2*)(x + (size_t)(sp + 1024) * D_ + d0);
    gav[tp] = *(const u32x2*)(mod + 1024 + d0);
    sbv[tp] = *(const u32x2*)(mod + 1536 + d0);
    cbv[tp] = *(const u32x2*)(mod + 2048 + d0);
  }
  u32x4 c1[2], c2[2][2][2];
  float twc[2][4], tws[2][4];
#pragma unroll
  for (int e = 0; e < 4; ++e) {
    const float a = (float)(((4 * g + e) * c) & 15) * (1.f / 16.f);
    const float cs = cosr(a), sn = sinr(a);
    c1[0][e] = pk(cs, -sn); c1[1][e] = pk(sn, cs);
  }
#pragma unroll
  for (int tp = 0; tp < 2; ++tp)
#pragma unroll
    for (int t = 0; t < 2; ++t)
#pragma unroll
      for (int e = 0; e < 4; ++e) {
        const float a = (float)(((16 * t + 4 * g + e) * (16 * tp + c)) & 31) * (1.f / 32.f);
        const float cs = cosr(a), sn = sinr(a);
        c2[tp][0][t][e] = pk(cs, -sn); c2[tp][1][t][e] = pk(sn, cs);
      }
#pragma unroll
  for (int t = 0; t < 2; ++t)
#pragma unroll
    for (int e = 0; e < 4; ++e) {
      const float a = (float)((16 * t + 4 * g + e) * c) * (1.f / 512.f);
      twc[t][e] = cosr(a); tws[t][e] = sinr(a);
    }
#pragma unroll
  for (int rep = 0; rep < 2; ++rep) {
    const int idx = tid + 512 * rep, p = idx >> 2, col = (idx >> 1) & 1, ih = idx & 1;
    const int k = col ? (p ? 512 - p : 256) : p;
    const int pos = (k >> 5) * 36 + (k & 31);
#pragma unroll
    for (int e = 0; e < 4; ++e) img[(4 * ih + e) * RS + pos] = rep ? in1[e] : in0[e];
  }
  __syncthreads();
  const f32x4 z = {0.f, 0.f, 0.f, 0.f};
  u32x4 p1[2];
#pragma unroll
  for (int t = 0; t < 2; ++t) {
    u32x4 af;
#pragma unroll
    for (int e = 0; e < 4; ++e) af[e] = img[w * RS + (4 * g + e) * 36 + 16 * t + c];
    const f32x4 re = mfma(af, c1[0], z), im = mfma(af, c1[1], z);
#pragma unroll
    for (int e = 0; e < 4; ++e)
      p1[t][e] = pk(re[e] * twc[t][e] - im[e] * tws[t][e], im[e] * twc[t][e] + re[e] * tws[t][e]);
  }
  float x1[2][8];
#pragma unroll
  for (int tp = 0; tp < 2; ++tp) {
    f32x4 re = mfma(p1[0], c2[tp][0][0], z), im = mfma(p1[0], c2[tp][1][0], z);
    re = mfma(p1[1], c2[tp][0][1], re); im = mfma(p1[1], c2[tp][1][1], im);
#pragma unroll
    for (int e = 0; e < 4; ++e) {
      const float ga = (e & 1) ? hi(gav[tp][e >> 1]) : lo(gav[tp][e >> 1]);
      const float xa = (e & 1) ? hi(xv[0][tp][e >> 1]) : lo(xv[0][tp][e >> 1]);
      const float xb = (e & 1) ? hi(xv[1][tp][e >> 1]) : lo(xv[1][tp][e >> 1]);
      const unsigned y = pk(re[e], im[e]);
      const unsigned gy = pk(ga * lo(y), ga * hi(y));
      const unsigned s = pk(xa + lo(gy), xb + hi(gy));
      x1[0][4 * tp + e] = lo(s); x1[1][4 * tp + e] = hi(s);
    }
  }
#pragma unroll
  for (int half = 0; half < 2; ++half) {
    float m, iv;
    ln_stats(x1[half], m, iv);
    const int row = sp + 1024 * half;
#pragma unroll
    for (int tp = 0; tp < 2; ++tp) {
      u32x2 xo, ho;
#pragma unroll
      for (int e2 = 0; e2 < 2; ++e2) {
        const float v0 = x1[half][4 * tp + 2 * e2], v1 = x1[half][4 * tp + 2 * e2 + 1];
        xo[e2] = pk(v0, v1);
        const unsigned c1p = pk(1.f + lo(cbv[tp][e2]), 1.f + hi(cbv[tp][e2]));
        const unsigned d = pk(v0 - m, v1 - m);
        const unsigned n = pk(lo(d) * iv, hi(d) * iv);
        const unsigned mm = pk(lo(n) * lo(c1p), hi(n) * hi(c1p));
        ho[e2] = pk(lo(mm) + lo(sbv[tp][e2]), hi(mm) + hi(sbv[tp][e2]));
      }
      *(u32x2*)(ws + OFF_X1 + ((size_t)row * D_ + 256 * tp + 16 * c + 4 * g) * 2) = xo;
      const int ks = 8 * tp + (c >> 1), gp = 2 * (c & 1) + (g >> 1);
      *(u32x2*)(ws + OFF_H2 + (size_t)(c3 * 16 + ks) * 1024 + (gp * 16 + w + 8 * half) * 16 + (g & 1) * 8) = ho;
    }
  }
}

__global__ __launch_bounds__(256, 1) void k4_gemm1(char* __restrict__ ws) {
  extern __shared__ __attribute__((aligned(16))) char dsm[];
  u32x4* As = (u32x4*)dsm;
  const int tid = threadIdx.x, w = __builtin_amdgcn_readfirstlane(tid >> 6), l = tid & 63;
  const int xcd = blockIdx.x & 7, T = xcd * 30 + (xcd < 2 ? xcd : 2) + (blockIdx.x >> 3);
  const int hh = T >= 121, T2 = T - 121 * hh, pn = T2 / 11, pm = 11 * hh + (T2 - 11 * pn);
  const int wc = 4 * pn + w, wcl = wc < 43 ? wc : 42;
  const u32x4* hsrc = (const u32x4*)(ws + OFF_H2) + l;
  const u32x4* wsrc = (const u32x4*)(ws + OFF_WGU) + (size_t)wcl * 16 * 4 * 64 + l;
  u32x4 ar[24], wf[16][4];
  auto loadA = [&](int i) {
    const int phi = 4 * i + w, ks = phi / 6, tt = phi - 6 * ks;
    int tile = 6 * pm + tt; tile = tile < 128 ? tile : 127;
    ar[i] = hsrc[(tile * 16 + ks) * 64];
  };
  auto loadW = [&](int ks) {
#pragma unroll
    for (int f = 0; f < 4; ++f) wf[ks][f] = wsrc[(ks * 4 + f) * 64];
  };
#pragma unroll
  for (int i = 0; i < 6; ++i) loadA(i);
#pragma unroll
  for (int ks = 0; ks < 4; ++ks) loadW(ks);
#pragma unroll
  for (int i = 6; i < 12; ++i) loadA(i);
#pragma unroll
  for (int ks = 4; ks < 8; ++ks) loadW(ks);
#pragma unroll
  for (int i = 12; i < 24; ++i) loadA(i);
  f32x4 acc[4][6];
#pragma unroll
  for (int f = 0; f < 4; ++f)
#pragma unroll
    for (int tt = 0; tt < 6; ++tt) acc[f][tt] = (f32x4){0.f, 0.f, 0.f, 0.f};
#pragma unroll
  for (int qd = 0; qd < 4; ++qd) {
#pragma unroll
    for (int i = 6 * qd; i < 6 * qd + 6; ++i) As[(4 * i + w) * 64 + l] = ar[i];
    __syncthreads();
#pragma unroll
    for (int ks = 4 * qd; ks < 4 * qd + 4; ++ks) {
      if (ks + 8 < 16) loadW(ks + 8);
      u32x4 bfr[6];
#pragma unroll
      for (int tt = 0; tt < 6; ++tt) bfr[tt] = As[(ks * 6 + tt) * 64 + l];
#pragma unroll
      for (int tt = 0; tt < 6; ++tt)
#pragma unroll
        for (int f = 0; f < 4; ++f) acc[f][tt] = mfma(wf[ks][f], bfr[tt], acc[f][tt]);
    }
  }
  if (wc < 43) {
#pragma unroll
    for (int tt = 0; tt < 6; ++tt) {
      u32x4 o;
#pragma unroll
      for (int ft = 0; ft < 2; ++ft)
#pragma unroll
        for (int e2 = 0; e2 < 2; ++e2) {
          const unsigned gg = pk(acc[ft][tt][2 * e2], acc[ft][tt][2 * e2 + 1]);
          const unsigned uu = pk(acc[2 + ft][tt][2 * e2], acc[2 + ft][tt][2 * e2 + 1]);
          o[2 * ft + e2] = pk(lo(gg) * lo(uu), hi(gg) * hi(uu));
        }
      const int tile = 6 * pm + tt;
      if (tile < 128) *((u32x4*)(ws + OFF_GU) + ((size_t)tile * 43 + wc) * 64 + l) = o;
    }
  }
}

__global__ __launch_bounds__(256, 1) void k5_gemm2(const char* __restrict__ ws, u16* __restrict__ out) {
  extern __shared__ __attribute__((aligned(16))) char dsm[];
  f32x4* red = (f32x4*)dsm;
  const int tid = threadIdx.x, w = __builtin_amdgcn_readfirstlane(tid >> 6), l = tid & 63, c = l & 15, g = l >> 4;
  const int xcd = blockIdx.x & 7, loc = blockIdx.x >> 3;
  const int tg = xcd * 4 + (loc >> 3), dg = loc & 7;
  const u32x4* gsrc = (const u32x4*)(ws + OFF_GU) + (size_t)tg * 4 * 43 * 64 + l;
  const u32x4* dsrc = (const u32x4*)(ws + OFF_WD) + (size_t)dg * 43 * 4 * 64 + l;
  const int Tt = 4 * tg + w;
  const int row = 32 * (8 * (Tt >> 5) + (c & 7)) + (Tt & 31) + 1024 * (c >> 3);
  u32x4 a[11][4], b[11][4];
  auto load = [&](int i) {
    int ks = 4 * i + w; ks = ks < 43 ? ks : 42;
#pragma unroll
    for (int j = 0; j < 4; ++j) { a[i][j] = dsrc[(ks * 4 + j) * 64]; b[i][j] = gsrc[(j * 43 + ks) * 64]; }
  };
#pragma unroll
  for (int i = 0; i < 7; ++i) load(i);
  u32x4 xr[2], gbv[2];
#pragma unroll
  for (int tp = 0; tp < 2; ++tp) {
    xr[tp] = *(const u32x4*)(ws + OFF_X1 + ((size_t)row * D_ + 64 * dg + 32 * tp + 8 * g) * 2);
    gbv[tp] = *(const u32x4*)(ws + OFF_MOD + (2560 + 64 * dg + 32 * tp + 8 * g) * 2);
  }
  f32x4 acc[4][4];
#pragma unroll
  for (int j = 0; j < 4; ++j)
#pragma unroll
    for (int tt = 0; tt < 4; ++tt) acc[j][tt] = (f32x4){0.f, 0.f, 0.f, 0.f};
#pragma unroll
  for (int i = 0; i < 11; ++i) {
    if (i + 7 < 11) load(i + 7);
    if (i < 10 || w < 3) {
#pragma unroll
      for (int tt = 0; tt < 4; ++tt)
#pragma unroll
        for (int j = 0; j < 4; ++j) acc[j][tt] = mfma(a[i][j], b[i][tt], acc[j][tt]);
    }
  }
  f32x4 mine[4];
#pragma unroll
  for (int tt = 0; tt < 4; ++tt) {
    if (tt != w) {
#pragma unroll
      for (int j = 0; j < 4; ++j) red[((tt * 4 + w) * 4 + j) * 64 + l] = acc[j][tt];
    } else {
#pragma unroll
      for (int j = 0; j < 4; ++j) mine[j] = acc[j][tt];
    }
  }
  __syncthreads();
#pragma unroll
  for (int sw = 0; sw < 4; ++sw)
    if (sw != w) {
#pragma unroll
      for (int j = 0; j < 4; ++j) mine[j] += red[((w * 4 + sw) * 4 + j) * 64 + l];
    }
#pragma unroll
  for (int tp = 0; tp < 2; ++tp) {
    u32x4 o;
#pragma unroll
    for (int m = 0; m < 4; ++m) {
      const f32x4 v = mine[2 * tp + (m >> 1)];
      const unsigned y = pk(v[2 * (m & 1)], v[2 * (m & 1) + 1]);
      const unsigned gy = pk(lo(gbv[tp][m]) * lo(y), hi(gbv[tp][m]) * hi(y));
      o[m] = pk(lo(xr[tp][m]) + lo(gy), hi(xr[tp][m]) + hi(gy));
    }
    *(u32x4*)(out + (size_t)row * D_ + 64 * dg + 32 * tp + 8 * g) = o;
  }
}

extern "C" void kernel_launch(void* const* d_in, const int* in_sizes, int n_in,
                              void* d_out, int out_size, void* d_ws, size_t ws_size,
                              hipStream_t stream) {
  const u16* x = (const u16*)d_in[0];
  char* ws = (char*)d_ws;
  (void)hipFuncSetAttribute((const void*)k4_gemm1, hipFuncAttributeMaxDynamicSharedMemorySize, 96 * 1024);
  (void)hipFuncSetAttribute((const void*)k5_gemm2, hipFuncAttributeMaxDynamicSharedMemorySize, 64 * 1024);
  k0_prep<<<289, 256, 0, stream>>>((const u16*)d_in[7], (const u16*)d_in[8], (const u16*)d_in[1], (const u16*)d_in[2],
                                   (const u16*)d_in[4], (const u16*)d_in[5], (const u16*)d_in[6], ws);
  k1_rowfft<<<128, 512, 0, stream>>>(x, ws);
  k2_colfft<<<257, 256, 0, stream>>>(ws);
  k3_rowifft<<<128, 512, 0, stream>>>(x, ws);
  k4_gemm1<<<242, 256, 96 * 1024, stream>>>(ws);
  k5_gemm2<<<256, 256, 64 * 1024, stream>>>(ws, (u16*)d_out);
}
```

```cpp
#include <hip/hip_runtime.h>
#include <hip/hip_bf16.h>
#include <stdint.h>

typedef __attribute__((ext_vector_type(8))) __bf16 bf16x8;
typedef __attribute__((ext_vector_type(2))) __bf16 bf16x2;
typedef __attribute__((ext_vector_type(4))) short s16x4;
typedef __attribute__((ext_vector_type(4))) float f32x4;
typedef __attribute__((ext_vector_type(2))) float f32x2;
typedef __attribute__((ext_vector_type(4))) unsigned u32x4;
typedef __attribute__((ext_vector_type(2))) unsigned u32x2;
typedef unsigned short u16;
typedef __amdgpu_buffer_rsrc_t rsrc_t;

#define DI __device__ __forceinline__
#define LAS __attribute__((address_space(3)))

#define STAMP(slot)
#define STAMP_DECL
#define STAMP_FLUSH()

constexpr int D_ = 512, F_ = 1376, DF = 257;
constexpr unsigned SENT = 0xFFFFFFFFu;
constexpr unsigned SPIN_CAP = 1u << 15;

constexpr unsigned OFF_EP   = 0;
constexpr unsigned OFF_GA   = 4096;
constexpr unsigned OFF_GT   = 8192;
constexpr unsigned OFF_FX   = 12288;
constexpr unsigned OFF_MODR = 16384;
constexpr unsigned OFF_FD   = 32768;
constexpr unsigned OFF_FE   = 65536;
constexpr unsigned OFF_WS0  = 98304;
constexpr unsigned OFF_WT   = 131072;
constexpr unsigned OFF_WGU  = OFF_WT + 257u * 2048u * 4u;
constexpr unsigned OFF_WD   = OFF_WGU + 86u * 16u * 2u * 1024u;
constexpr unsigned OFF_T1   = OFF_WD + 8u * 43u * 4u * 1024u;
constexpr unsigned OFF_T2   = OFF_T1 + (2u << 20);
constexpr unsigned OFF_H2   = OFF_T2 + (2u << 20);
constexpr unsigned OFF_X1   = OFF_H2 + (2u << 20);
constexpr unsigned OFF_GU   = OFF_X1 + (2u << 20);
constexpr unsigned WS_BYTES = OFF_GU + 64u * 86u * 1024u;

constexpr int L_RED = 0, L_MODA = 2048, L_IMG1 = 4096, L_INB = 12544, L_EX = 20736;
constexpr int L_RED2 = 28928, L_CNT = 31744, L_HS = 32768, L_REV = 73728;
constexpr int L_IMG3 = 0, L_H2 = 12544, L_X1 = 20736, L_AS = 32768;
constexpr int LDS_BYTES = 131072;

#define HDELAY 12
#define CW1 4
#define CW2 7
constexpr int CW3 = 10, GWIN = 4, PF = 4;

DI unsigned pk(float a, float b) {
  f32x2 v = {a, b};
  return __builtin_bit_cast(unsigned, __builtin_convertvector(v, bf16x2));
}
DI float lo(unsigned u) { return __uint_as_float(u << 16); }
DI float hi(unsigned u) { return __uint_as_float(u & 0xffff0000u); }
DI float rb(float a) { return lo(pk(a, a)); }
DI f32x4 mfma(u32x4 a, u32x4 b, f32x4 c) {
  return __builtin_amdgcn_mfma_f32_16x16x32_bf16(__builtin_bit_cast(bf16x8, a), __builtin_bit_cast(bf16x8, b), c, 0, 0, 0);
}
DI float cosr(float rev) { return __builtin_amdgcn_cosf(rev); }
DI float sinr(float rev) { return __builtin_amdgcn_sinf(rev); }

DI u32x4 ld16(rsrc_t r, unsigned v, unsigned s) { return __builtin_amdgcn_raw_buffer_load_b128(r, v, s, 16); }
DI u32x2 ld8(rsrc_t r, unsigned v, unsigned s) { return __builtin_amdgcn_raw_buffer_load_b64(r, v, s, 16); }
DI unsigned ld4(rsrc_t r, unsigned v, unsigned s) { return __builtin_amdgcn_raw_buffer_load_b32(r, v, s, 16); }
DI void st16(u32x4 x, rsrc_t r, unsigned v, unsigned s) { __builtin_amdgcn_raw_buffer_store_b128(x, r, v + s, 0, 16); }
DI void st4(unsigned x, rsrc_t r, unsigned v, unsigned s) { __builtin_amdgcn_raw_buffer_store_b32(x, r, v, s, 16); }

#define FENCE() asm volatile("" ::: "memory")
#define BAR() do { asm volatile("s_waitcnt lgkmcnt(0)" ::: "memory"); __builtin_amdgcn_s_barrier(); FENCE(); } while (0)
#define DRAIN() asm volatile("s_waitcnt vmcnt(0)" ::: "memory")
#define SB() __builtin_amdgcn_sched_barrier(0)
#define PIN(x) asm volatile("" : "+v"(x))

DI void glds16(const void* src, unsigned lds_base) {
  unsigned keep;
  asm volatile("s_mov_b32 %0, m0\n\ts_mov_b32 m0, %2\n\ts_nop 0\n\tglobal_load_lds_dwordx4 %1, off\n\ts_mov_b32 m0, %0"
               : "=&s"(keep) : "v"(src), "s"(lds_base) : "memory");
}
DI unsigned umax3(unsigned a, unsigned b, unsigned c) { return max(max(a, b), c); }
DI unsigned vmax(u32x4 v, unsigned m) { return umax3(v[2], v[3], umax3(v[0], v[1], m)); }
DI bool wave_any(bool p) { return __builtin_amdgcn_ballot_w64(p) != 0ull; }

template <int CTRL> DI float dpp_add(float v) {
  int s = __builtin_amdgcn_update_dpp(0, __float_as_int(v), CTRL, 0xf, 0xf, true);
  return v + __int_as_float(s);
}
DI float row_sum(float v) { v = dpp_add<0xB1>(v); v = dpp_add<0x4E>(v); v = dpp_add<0x141>(v); return dpp_add<0x140>(v); }
DI float wave_sum(float v) {
  const int i = __float_as_int(row_sum(v));
  const float a = __int_as_float(__builtin_amdgcn_readlane(i, 0)), b = __int_as_float(__builtin_amdgcn_readlane(i, 16));
  const float c = __int_as_float(__builtin_amdgcn_readlane(i, 32)), d = __int_as_float(__builtin_amdgcn_readlane(i, 48));
  return (a + b) + (c + d);
}
DI void ln_stats(const float (&x)[8], float& mean_b, float& inv_sd) {
  float s = ((x[0] + x[1]) + (x[2] + x[3])) + ((x[4] + x[5]) + (x[6] + x[7]));
  const float mean = wave_sum(s) * (1.f / 512.f);
  float q0 = 0.f, q1 = 0.f;
#pragma unroll
  for (int i = 0; i < 4; ++i) { const float d = x[i] - mean, e = x[i + 4] - mean; q0 += d * d; q1 += e * e; }
  const float var = rb(wave_sum(q0 + q1) * (1.f / 512.f));
  const float sd = rb(__builtin_amdgcn_sqrtf(rb(var + 1e-5f)));
  mean_b = rb(mean);
  inv_sd = __builtin_amdgcn_rcpf(sd);
}
DI float silu_b(unsigned bits) {
  const float c = lo(bits);
  return rb(c * __builtin_amdgcn_rcpf(1.f + __expf(-c)));
}
DI u32x4 twiddle(f32x4 re, f32x4 im, f32x4 C, f32x4 S) {
  const f32x4 r = re * C + im * S, i = im * C - re * S;
  return (u32x4){pk(r[0], i[0]), pk(r[1], i[1]), pk(r[2], i[2]), pk(r[3], i[3])};
}
DI void dft_frag(float base, float step, u32x4& fre, u32x4& fim) {
#pragma unroll
  for (int e = 0; e < 4; ++e) {
    const float a = base + (float)e * step, cs = cosr(a), sn = sinr(a);
    fre[e] = pk(cs, sn); fim[e] = pk(-sn, cs);
  }
}
DI void tw_frag(float base, float step, f32x4& C, f32x4& S) {
#pragma unroll
  for (int e = 0; e < 4; ++e) { const float a = base + (float)e * step; C[e] = cosr(a); S[e] = sinr(a); }
}
DI u32x2 tr_read(const char* p) {
  return __builtin_bit_cast(u32x2, __builtin_amdgcn_ds_read_tr16_b64_v4i16((LAS s16x4*)p));
}

DI void wait_flags(rsrc_t R, unsigned voff, unsigned soff, unsigned tag, bool active) {
  for (unsigned spins = 0;; ++spins) {
    FENCE();
    const unsigned v = ld4(R, voff, soff);
    if (!wave_any(active && v != tag) || spins > SPIN_CAP) break;
    __builtin_amdgcn_s_sleep(2);
  }
  FENCE();
}

__global__ __launch_bounds__(512, 2) void fused_block(const u16* __restrict__ x, const u16* __restrict__ wr,
                                                      const u16* __restrict__ wi, const u16* __restrict__ wg,
                                                      const u16* __restrict__ wu, const u16* __restrict__ wd,
                                                      const u16* __restrict__ cvec, const u16* __restrict__ wmod,
                                                      u16* __restrict__ out, char* __restrict__ ws) {
  extern __shared__ __attribute__((aligned(16))) char sm[];
  const int b = blockIdx.x, tid = threadIdx.x, w8 = __builtin_amdgcn_readfirstlane(tid >> 6), l = tid & 63, c = l & 15, g = l >> 4;
  const int w = w8 & 3, t4 = tid & 255;
  const bool crit = w8 < 4;
  const rsrc_t R = __builtin_amdgcn_make_buffer_rsrc(ws, 0, WS_BYTES, 0x00020000);
  const rsrc_t RM = __builtin_amdgcn_make_buffer_rsrc((void*)wmod, 0, 512 * 3072 * 2, 0x00020000);
  const f32x4 z4 = {0.f, 0.f, 0.f, 0.f};
  const unsigned lds0 = (unsigned)(size_t)(LAS char*)sm;
  STAMP_DECL;
  STAMP(0);

  const int xcd = b & 7, loc = b >> 3;
  const bool g1_on = loc < 30 + (xcd < 2);
  const int T1i = g1_on ? xcd * 30 + (xcd < 2 ? xcd : 2) + loc : 0;
  const int hh = T1i >= 121, T1j = T1i - 121 * hh, pn = T1j / 11, pm = 11 * hh + (T1j - 11 * pn);
  const int ft = 8 * pn + w8;
  const unsigned wgu_base = OFF_WGU + min(ft, 85) * 32768;
  u32x4 wf[16][2];
  auto loadW = [&](int ks) __attribute__((always_inline)) {
    wf[ks][0] = ld16(R, l * 16 + (ks & 1) * 2048, wgu_base + (ks >> 1) * 4096);
    wf[ks][1] = ld16(R, l * 16 + (ks & 1) * 2048 + 1024, wgu_base + (ks >> 1) * 4096);
  };
  unsigned tag;

  if (crit) {
    const u32x4 SENT4 = {SENT, SENT, SENT, SENT};
    st16(SENT4, R, t4 * 16, OFF_T1 + b * 8192);
    st16(SENT4, R, t4 * 16 + 4096, OFF_T1 + b * 8192);
    st16(SENT4, R, t4 * 16, OFF_T2 + b * 8192);
    st16(SENT4, R, t4 * 16 + 4096, OFF_T2 + b * 8192);
    FENCE();
    const unsigned ep_v = ld4(R, 0, OFF_EP);
    const u32x2 cv = *(const u32x2*)(cvec + 4 * (t4 >> 1));
    const int sp1 = 32 * (8 * ((b >> 5) & 3) + 2 * ((b >> 3) & 3) + (w >> 1)) + 16 * (b >> 7) + 2 * (b & 7) + (w & 1);
    unsigned ua[4], ub[4];
    {
      const unsigned* xa = (const unsigned*)(x + (size_t)sp1 * D_) + 64 * g + c;
#pragma unroll
      for (int e = 0; e < 4; ++e) { ua[e] = xa[16 * e]; ub[e] = xa[16 * e + 1024 * D_ / 2]; }
    }
    FENCE();
    const int slice = (b & 7) * 8 + (b >> 3);
    u32x4 wm[4];
    if (b < 64) {
#pragma unroll
      for (int i = 0; i < 4; ++i) wm[i] = __builtin_amdgcn_raw_buffer_load_b128(RM, (t4 >> 1) * 24576 + (t4 & 1) * 16, slice * 32 + i * 6144, 0);
    }
    if (tid == 0) *(unsigned*)(sm + L_CNT) = 0u;
    FENCE();
    const float fc = (float)c, fg4 = (float)(4 * g), fj1 = (float)(16 * w + c);
    u32x4 k16[2], k32a[2][2][2], k32[2][2][2];
    f32x4 tw1c[2], tw1s[2], t1c[2], t1s[2];
    dft_frag(fg4 * fc * (1.f / 16.f), fc * (1.f / 16.f), k16[0], k16[1]);
#pragma unroll
    for (int tp = 0; tp < 2; ++tp) {
      const float v = (fc + 16.f * tp) * (1.f / 32.f);
#pragma unroll
      for (int t = 0; t < 2; ++t) {
        dft_frag((2.f * fg4 + t) * v, 2.f * v, k32a[tp][0][t], k32a[tp][1][t]);
        PIN(k32a[tp][0][t]); PIN(k32a[tp][1][t]);
      }
    }
#pragma unroll
    for (int t = 0; t < 2; ++t) {
      tw_frag((2.f * fg4 + t) * fc * (1.f / 512.f), fc * (1.f / 256.f), tw1c[t], tw1s[t]);
      PIN(tw1c[t]); PIN(tw1s[t]);
    }
    PIN(k16[0]); PIN(k16[1]);
    float sc[4];
#pragma unroll
    for (int i = 0; i < 4; ++i) { sc[i] = silu_b((i & 1) ? cv[i >> 1] >> 16 : cv[i >> 1]); PIN(sc[i]); }
    tag = __builtin_amdgcn_readfirstlane(ep_v) + 1u;
    FENCE();
    STAMP(1);
    if (b < 64) {
      float a[8];
#pragma unroll
      for (int i = 0; i < 4; ++i) {
#pragma unroll
        for (int m = 0; m < 4; ++m) {
          a[2 * m] = (i ? a[2 * m] : 0.f) + sc[i] * lo(wm[i][m]);
          a[2 * m + 1] = (i ? a[2 * m + 1] : 0.f) + sc[i] * hi(wm[i][m]);
        }
      }
#pragma unroll
      for (int e = 0; e < 8; ++e) a[e] = dpp_add<0x128>(dpp_add<0x124>(dpp_add<0x122>(a[e])));
      if (c < 2) {
        ((f32x4*)(sm + L_RED))[(t4 >> 4) * 4 + 2 * c] = (f32x4){a[0], a[1], a[2], a[3]};
        ((f32x4*)(sm + L_RED))[(t4 >> 4) * 4 + 2 * c + 1] = (f32x4){a[4], a[5], a[6], a[7]};
      }
    }
    if (tid == 0) *(unsigned*)(sm + L_CNT + 4) = tag;
    BAR();
    if (w == 0) {
      if (l == 0) st4(tag, R, 0, OFF_GT + b * 16);
      if (b < 64) {
        f32x4 s = ((const f32x4*)(sm + L_RED))[c * 4 + g];
#pragma unroll
        for (int e = 0; e < 4; ++e) s[e] = row_sum(s[e]);
        if (c == 0) st16((u32x4){pk(s[0], s[1]), tag, pk(s[2], s[3]), tag}, R, g * 16, OFF_GA + slice * 64);
      }
    }
    FENCE();
    STAMP(2);
    float na[8], nb[8];
    {
#pragma unroll
      for (int e = 0; e < 4; ++e) { na[2 * e] = lo(ua[e]); na[2 * e + 1] = hi(ua[e]); nb[2 * e] = lo(ub[e]); nb[2 * e + 1] = hi(ub[e]); }
      float ma, ia, mb, ib;
      ln_stats(na, ma, ia);
      ln_stats(nb, mb, ib);
#pragma unroll
      for (int e = 0; e < 8; ++e) { na[e] = (na[e] - ma) * ia; nb[e] = (nb[e] - mb) * ib; PIN(na[e]); PIN(nb[e]); }
    }
#pragma unroll
    for (int tp = 0; tp < 2; ++tp) {
      const float v = (fc + 16.f * tp) * (1.f / 32.f);
#pragma unroll
      for (int t = 0; t < 2; ++t) {
        dft_frag((fg4 + 16.f * t) * v, v, k32[tp][0][t], k32[tp][1][t]);
        PIN(k32[tp][0][t]); PIN(k32[tp][1][t]);
      }
    }
    u32x4 k1c[4][2], k4c[4][2];
    f32x4 t3c[2], t3s[2];
    auto gen_p2 = [&]() __attribute__((always_inline)) {
    {
#pragma unroll
      for (int t = 0; t < 2; ++t) {
        tw_frag((fg4 + 16.f * t) * fj1 * (1.f / 2048.f), fj1 * (1.f / 2048.f), t1c[t], t1s[t]);
        PIN(t1c[t]); PIN(t1s[t]);
      }
      const float sg = (c & 1) ? -1.f : 1.f;
      float gr[8], gi[8];
#pragma unroll
      for (int i = 0; i < 8; ++i) {
        const float a = (0.5f * fg4 + (float)(8 * (i >> 1) + (i & 1))) * (fj1 * (1.f / 64.f));
        gr[i] = cosr(a); gi[i] = -sinr(a);
      }
      if (b != 0) {
#pragma unroll
        for (int i = 0; i < 8; ++i) {
          const float A = 0.5f * (gr[i] + sg * gi[i]), B = 0.5f * (gr[i] - sg * gi[i]);
          k1c[i >> 1][0][i & 1] = pk(A, sg * B); k1c[i >> 1][0][2 + (i & 1)] = pk(B, sg * A);
          k1c[i >> 1][1][i & 1] = pk(-sg * B, A); k1c[i >> 1][1][2 + (i & 1)] = pk(sg * A, -B);
        }
      } else {
#pragma unroll
        for (int i = 0; i < 8; ++i) {
          k1c[i >> 1][0][i & 1] = pk(gr[i], sg * gr[i]); k1c[i >> 1][0][2 + (i & 1)] = pk(-gi[i], -sg * gi[i]);
          k1c[i >> 1][1][i & 1] = pk(gi[i], sg * gi[i]); k1c[i >> 1][1][2 + (i & 1)] = pk(gr[i], sg * gr[i]);
        }
      }
#pragma unroll
      for (int i = 0; i < 8; ++i) PIN(k1c[i >> 1][i & 1]);
    }
    {
#pragma unroll
      for (int t = 0; t < 2; ++t) {
        const float v = (fc + 16.f * t) * (1.f / 2048.f);
        tw_frag(((float)(16 * w) + fg4) * v, v, t3c[t], t3s[t]);
        PIN(t3c[t]); PIN(t3s[t]);
      }
      const int n1p = 8 * w + (c & 7);
      const bool col1 = c >= 8;
      float gr[16], gi[16];
#pragma unroll
      for (int i = 0; i < 16; ++i) {
        const float a = (fg4 + (float)(16 * (i >> 2) + (i & 3))) * ((float)n1p * (1.f / 64.f));
        gr[i] = cosr(a); gi[i] = -sinr(a);
      }
      const float cs1 = col1 ? -1.f : 1.f;
      if (b != 0) {
#pragma unroll
        for (int i = 0; i < 16; ++i) {
          const float s = (i & 1) ? -cs1 : cs1;
          const float U = gr[i] + s * gi[i], V = gr[i] - s * gi[i];
          k4c[i >> 2][0][i & 3] = pk(U, s * V);
          k4c[i >> 2][1][i & 3] = pk(-cs1 * s * V, cs1 * U);
        }
      } else {
#pragma unroll
        for (int i = 0; i < 16; ++i) {
          const float s = (i & 1) ? -1.f : 1.f;
          const float U = col1 ? -gi[i] : gr[i], V = col1 ? -gr[i] : -gi[i];
          k4c[i >> 2][0][i & 3] = pk(U, V);
          k4c[i >> 2][1][i & 3] = pk(-s * U, -s * V);
        }
      }
#pragma unroll
      for (int i = 0; i < 8; ++i) PIN(k4c[i >> 1][i & 1]);
    }
    };
    if (b >= 64) gen_p2();
    STAMP(3);
    {
      u32x4 v;
      for (unsigned spins = 0;; ++spins) {
        FENCE();
        v = ld16(R, t4 * 16, OFF_GA);
        const unsigned t = ld4(R, t4 * 16, OFF_GT);
        if (!wave_any(v[1] != tag || v[3] != tag || t != tag) || spins > SPIN_CAP) break;
        __builtin_amdgcn_s_sleep(6);
      }
      *(u32x2*)(sm + L_MODA + t4 * 8) = (u32x2){v[0], v[2]};
      BAR();
    }
    if (tid == 0 && b == 0) st4(tag, R, 0, OFF_EP);
    STAMP(4);

    {
      const unsigned* moda = (const unsigned*)(sm + L_MODA) + 64 * g + c;
      u32x4 af[2];
#pragma unroll
      for (int e = 0; e < 4; ++e) {
        const unsigned us = moda[16 * e], uc = moda[256 + 16 * e];
#pragma unroll
        for (int t = 0; t < 2; ++t) {
          const float ca1 = 1.f + (t ? hi(uc) : lo(uc)), sa = t ? hi(us) : lo(us);
          af[t][e] = pk(na[2 * e + t] * ca1 + sa, nb[2 * e + t] * ca1 + sa);
        }
      }
      u32x4 p1[2];
#pragma unroll
      for (int t = 0; t < 2; ++t) p1[t] = twiddle(mfma(af[t], k16[0], z4), mfma(af[t], k16[1], z4), tw1c[t], tw1s[t]);
      unsigned* img = (unsigned*)(sm + L_IMG1);
#pragma unroll
      for (int tp = 0; tp < 2; ++tp) {
        f32x4 re = mfma(k32a[tp][0][0], p1[0], z4), im = mfma(k32a[tp][1][0], p1[0], z4);
        re = mfma(k32a[tp][0][1], p1[1], re); im = mfma(k32a[tp][1][1], p1[1], im);
#pragma unroll
        for (int e = 0; e < 4; ++e) {
          const int kk = c + 64 * g + 16 * e;
          const int p = tp ? ((256 - kk) & 255) : kk;
          img[(4 * (w & 1) + 2 * tp + (w >> 1)) * 260 + p] = pk(re[e], im[e]);
        }
      }
      BAR();
#pragma unroll
      for (int rep = 0; rep < 2; ++rep) {
        const int p = (t4 >> 1) + 128 * rep, qq = t4 & 1;
        u32x4 o;
#pragma unroll
        for (int e = 0; e < 4; ++e) o[e] = img[(4 * qq + e) * 260 + p];
        st16(o, R, p * 8192 + qq * 16, OFF_T1 + b * 32);
      }
    }
    FENCE();
    STAMP(5);
    if (b < 64) gen_p2();
    const int j0 = 16 * w + c + 256 * g;
    const float nrm = 1.f / (2048.f * 512.f);
    f32x4 wpr[2], wpi[2], wmr[2], wmi[2];
    if (b == 0) {
      u32x2 q[2][4][4];
      for (unsigned spins = 0;; ++spins) {
        FENCE();
        bool bad = false;
#pragma unroll
        for (int i = 0; i < 8; ++i) {
          const int j = j0 + 64 * (16 * (i >> 2) + (i & 3)), jn = (2048 - j) & 2047;
          q[i >> 2][i & 3][0] = ld8(R, j * 8, OFF_WS0); q[i >> 2][i & 3][1] = ld8(R, jn * 8, OFF_WS0);
          q[i >> 2][i & 3][2] = ld8(R, j * 8, OFF_WS0 + 16384); q[i >> 2][i & 3][3] = ld8(R, jn * 8, OFF_WS0 + 16384);
        }
#pragma unroll
        for (int i = 0; i < 32; ++i) bad |= q[i >> 4][(i >> 2) & 3][i & 3][1] != tag;
        if (!wave_any(bad) || spins > SPIN_CAP) break;
        __builtin_amdgcn_s_sleep(8);
      }
#pragma unroll
      for (int i = 0; i < 8; ++i) {
        const unsigned a0 = q[i >> 2][i & 3][0][0], a1 = q[i >> 2][i & 3][1][0], b0 = q[i >> 2][i & 3][2][0], b1 = q[i >> 2][i & 3][3][0];
        const float sc4 = 0.25f * nrm;
        const float ar = (lo(a0) + lo(a1)) * sc4, ai = (hi(a0) - hi(a1)) * sc4;
        const float br = (lo(b0) + lo(b1)) * sc4, bi = (hi(b0) - hi(b1)) * sc4;
        wpr[i >> 2][i & 3] = ar + br; wpi[i >> 2][i & 3] = ai + bi; wmr[i >> 2][i & 3] = ar - br; wmi[i >> 2][i & 3] = ai - bi;
      }
#pragma unroll
      for (int tp = 0; tp < 2; ++tp) { PIN(wpr[tp]); PIN(wpi[tp]); PIN(wmr[tp]); PIN(wmi[tp]); }
    }
    DRAIN();
    STAMP(6);
    unsigned wv[8];
    {
      u32x4 v[2];
      for (unsigned spins = 0;; ++spins) {
        FENCE();
        v[0] = ld16(R, t4 * 16, OFF_T1 + b * 8192);
        v[1] = ld16(R, t4 * 16 + 4096, OFF_T1 + b * 8192);
        if (!wave_any(vmax(v[0], vmax(v[1], 0u)) == SENT) || spins > SPIN_CAP) break;
        __builtin_amdgcn_s_sleep(2);
      }
      FENCE();
#pragma unroll
      for (int i = 0; i < 8; ++i) wv[i] = ld4(R, j0 * 4 + (i & 3) * 256, OFF_WT + b * 8192 + (i >> 2) * 4096);
      ((u32x4*)(sm + L_INB))[t4] = v[0];
      ((u32x4*)(sm + L_INB))[t4 + 256] = v[1];
    }
    STAMP(17);
    BAR();
    STAMP(7);

    {
      const u32x4* inb = (const u32x4*)(sm + L_INB) + l;
      u32x4 p1[2], p2[2];
#pragma unroll
      for (int t = 0; t < 2; ++t) {
        f32x4 re = z4, im = z4;
#pragma unroll
        for (int ks = 0; ks < 4; ++ks) {
          const u32x4 d = inb[(t * 4 + ks) * 64];
          re = mfma(d, k1c[ks][0], re); im = mfma(d, k1c[ks][1], im);
        }
        p1[t] = twiddle(re, im, t1c[t], t1s[t]);
      }
      f32x4 xr[2], xi[2];
#pragma unroll
      for (int tp = 0; tp < 2; ++tp) {
        xr[tp] = mfma(k32[tp][0][0], p1[0], z4); xi[tp] = mfma(k32[tp][1][0], p1[0], z4);
        xr[tp] = mfma(k32[tp][0][1], p1[1], xr[tp]); xi[tp] = mfma(k32[tp][1][1], p1[1], xi[tp]);
      }
      if (b != 0) {
#pragma unroll
        for (int tp = 0; tp < 2; ++tp) {
          f32x4 a, bb;
#pragma unroll
          for (int e = 0; e < 4; ++e) { a[e] = lo(wv[4 * tp + e]); bb[e] = hi(wv[4 * tp + e]); }
          a *= nrm; bb *= -nrm;
          const f32x4 yr = xr[tp] * a + xi[tp] * bb, yi = xr[tp] * bb - xi[tp] * a;
#pragma unroll
          for (int e = 0; e < 4; ++e) p2[tp][e] = pk(yr[e], yi[e]);
        }
      } else {
        f32x2* rev = (f32x2*)(sm + L_REV);
#pragma unroll
        for (int tp = 0; tp < 2; ++tp)
#pragma unroll
          for (int e = 0; e < 4; ++e) rev[j0 + 64 * (16 * tp + e)] = (f32x2){xr[tp][e], xi[tp][e]};
        BAR();
#pragma unroll
        for (int tp = 0; tp < 2; ++tp) {
          f32x4 nr, ni;
#pragma unroll
          for (int e = 0; e < 4; ++e) { const f32x2 n = rev[(2048 - (j0 + 64 * (16 * tp + e))) & 2047]; nr[e] = n[0]; ni[e] = n[1]; }
          const f32x4 yr = xr[tp] * wpr[tp] - xi[tp] * wpi[tp] + nr * wmr[tp] + ni * wmi[tp];
          const f32x4 yi = ni * wmr[tp] - nr * wmi[tp] - xr[tp] * wpi[tp] - xi[tp] * wpr[tp];
#pragma unroll
          for (int e = 0; e < 4; ++e) p2[tp][e] = pk(yr[e], yi[e]);
        }
      }
      u32x4* ex = (u32x4*)(sm + L_EX);
#pragma unroll
      for (int t = 0; t < 2; ++t) {
        f32x4 re = mfma(p2[0], k32[t][0][0], z4), im = mfma(p2[0], k32[t][1][0], z4);
        re = mfma(p2[1], k32[t][0][1], re); im = mfma(p2[1], k32[t][1][1], im);
        ex[(w * 2 + t) * 64 + l] = twiddle(re, im, t3c[t], t3s[t]);
      }
      BAR();
#pragma unroll
      for (int t = 0; t < 2; ++t) {
        f32x4 re = z4, im = z4;
#pragma unroll
        for (int ks = 0; ks < 4; ++ks) {
          const u32x4 d = ex[(ks * 2 + t) * 64 + l];
          re = mfma(k4c[ks][0], d, re); im = mfma(k4c[ks][1], d, im);
        }
        u32x4 o;
#pragma unroll
        for (int e = 0; e < 4; ++e) o[e] = pk(re[e], im[e]);
        st16(o, R, ((2 * w + (g & 1)) * 32 + 16 * t + c) * 8192 + (g >> 1) * 16, OFF_T2 + b * 32);
      }
    }
    FENCE();
    STAMP(8);
    const int sp3 = 32 * (4 * (b >> 5) + w) + (b & 31);
    u32x2 xv[2][2], gav[2], sbv[2], cbv[2];
#pragma unroll
    for (int tp = 0; tp < 2; ++tp) {
      const int d0 = 256 * tp + 16 * c + 4 * g;
      xv[0][tp] = *(const u32x2*)(x + (size_t)sp3 * D_ + d0);
      xv[1][tp] = *(const u32x2*)(x + (size_t)(sp3 + 1024) * D_ + d0);
      gav[tp] = ld8(R, d0 * 2, OFF_MODR);
      sbv[tp] = ld8(R, d0 * 2, OFF_MODR + 1024);
      cbv[tp] = ld8(R, d0 * 2, OFF_MODR + 2048);
    }
#pragma unroll
    for (int ks = 0; ks < CW1; ++ks) loadW(ks);
    FENCE();
    f32x4 tw3c[2], tw3s[2];
#pragma unroll
    for (int t = 0; t < 2; ++t) {
      tw_frag((fg4 + 16.f * t) * fc * (1.f / 512.f), fc * (1.f / 512.f), tw3c[t], tw3s[t]);
      PIN(tw3c[t]); PIN(tw3s[t]);
    }
    DRAIN();
    STAMP(9);
    {
      u32x4 v[2];
      for (unsigned spins = 0;; ++spins) {
        FENCE();
        v[0] = ld16(R, t4 * 16, OFF_T2 + b * 8192);
        v[1] = ld16(R, t4 * 16 + 4096, OFF_T2 + b * 8192);
        if (!wave_any(vmax(v[0], vmax(v[1], 0u)) == SENT) || spins > SPIN_CAP) break;
        __builtin_amdgcn_s_sleep(2);
      }
      unsigned* img = (unsigned*)(sm + L_IMG3);
#pragma unroll
      for (int rep = 0; rep < 2; ++rep) {
        const int p = (t4 >> 1) + 128 * rep;
        const int k = (t4 & 1) ? (p ? 512 - p : 256) : p;
        const int pos = (k >> 5) * 36 + (k & 31);
#pragma unroll
        for (int e = 0; e < 4; ++e) img[e * 580 + pos] = v[rep][e];
      }
    }
#pragma unroll
    for (int ks = CW1; ks < CW2; ++ks) loadW(ks);
    BAR();
    STAMP(10);

    {
      const unsigned* img = (const unsigned*)(sm + L_IMG3) + w * 580 + 4 * g * 36 + c;
      u32x4 p1[2];
#pragma unroll
      for (int t = 0; t < 2; ++t) {
        u32x4 af;
#pragma unroll
        for (int e = 0; e < 4; ++e) af[e] = img[e * 36 + 16 * t];
        p1[t] = twiddle(mfma(af, k16[0], z4), mfma(af, k16[1], z4), tw3c[t], tw3s[t]);
      }
      float x1[2][8];
#pragma unroll
      for (int tp = 0; tp < 2; ++tp) {
        f32x4 re = mfma(p1[0], k32[tp][0][0], z4), im = mfma(p1[0], k32[tp][1][0], z4);
        re = mfma(p1[1], k32[tp][0][1], re); im = mfma(p1[1], k32[tp][1][1], im);
#pragma unroll
        for (int e = 0; e < 4; ++e) {
          const float ga = (e & 1) ? hi(gav[tp][e >> 1]) : lo(gav[tp][e >> 1]);
          const float xa = (e & 1) ? hi(xv[0][tp][e >> 1]) : lo(xv[0][tp][e >> 1]);
          const float xb = (e & 1) ? hi(xv[1][tp][e >> 1]) : lo(xv[1][tp][e >> 1]);
          x1[0][4 * tp + e] = xa + ga * re[e];
          x1[1][4 * tp + e] = xb - ga * im[e];
        }
      }
      float mean[2], isd[2];
      ln_stats(x1[0], mean[0], isd[0]);
      ln_stats(x1[1], mean[1], isd[1]);
#pragma unroll
      for (int half = 0; half < 2; ++half) {
        const float m = mean[half], iv = isd[half];
        const int rr = 4 * half + w;
#pragma unroll
        for (int tp = 0; tp < 2; ++tp) {
          u32x2 xo, ho;
#pragma unroll
          for (int e2 = 0; e2 < 2; ++e2) {
            const float v0 = x1[half][4 * tp + 2 * e2], v1 = x1[half][4 * tp + 2 * e2 + 1];
            xo[e2] = pk(v0, v1);
            ho[e2] = pk((v0 - m) * iv * (1.f + lo(cbv[tp][e2])) + lo(sbv[tp][e2]),
                        (v1 - m) * iv * (1.f + hi(cbv[tp][e2])) + hi(sbv[tp][e2]));
          }
          *(u32x2*)(sm + L_X1 + rr * 1024 + (256 * tp + 16 * c + 4 * g) * 2) = xo;
          const int kg = 4 * (8 * tp + (c >> 1)) + 2 * (c & 1) + (g >> 1);
          *(u32x2*)(sm + L_H2 + kg * 128 + ((rr + kg) & 7) * 16 + (g & 1) * 8) = ho;
        }
      }
    }
    BAR();
  } else {
    __builtin_amdgcn_s_sleep(HDELAY);
    const unsigned hs = L_HS + w * 10240;
    const int h3 = b * 3 + w;
    const int u0 = h3 < 608 ? 2 * h3 : 608 + h3;
    const int nu = w == 3 ? 0 : (h3 < 608 ? 2 : 1);
    const bool wd_on = w != 3 && h3 < 688, mr_on = b >= 64 && b < 128;
    const int mslice = (b & 7) * 8 + ((b >> 3) & 7);
    for (int i = 0; i < nu; ++i) {
      const int u = u0 + i, mat = u >= 688, v = u - 688 * mat, ks = v / 43, wc = v - 43 * ks;
      const u16* src = (mat ? wu : wg) + (size_t)(32 * ks + (l >> 2)) * F_ + 32 * wc + 8 * (l & 3);
      glds16(src, lds0 + hs + i * 2048);
      glds16(src + 16 * F_, lds0 + hs + i * 2048 + 1024);
    }
    u32x4 tail = {0u, 0u, 0u, 0u};
    if (w == 3) {
      tail = ((const u32x4*)(((l & 1) ? wi : wr) + (size_t)8 * b * DF))[256];
#pragma unroll
      for (int i = 0; i < 8; ++i) glds16((const u32x4*)(((i & 4) ? wi : wr) + (size_t)8 * b * DF) + (i & 3) * 64 + l, lds0 + hs + 2048 + i * 1024);
    }
    u32x4 wm[8], cv;
    if (mr_on) {
#pragma unroll
      for (int i = 0; i < 8; ++i) wm[i] = __builtin_amdgcn_raw_buffer_load_b128(RM, (t4 >> 2) * 49152 + (l & 3) * 16, 2048 + mslice * 64 + i * 6144, 0);
      cv = *(const u32x4*)(cvec + 8 * (t4 >> 2));
    }
    if (wd_on) {
      const u16* src = wd + (size_t)(32 * (h3 >> 4) + (l >> 2)) * D_ + 32 * (h3 & 15) + 8 * (l & 3);
      glds16(src, lds0 + hs + 4096);
      glds16(src + 16 * D_, lds0 + hs + 5120);
    }
    STAMP(1);
    BAR();
    STAMP(2);
    tag = __builtin_amdgcn_readfirstlane(*(const unsigned*)(sm + L_CNT + 4));
    DRAIN();
    STAMP(3);
    const int q = c >> 2, p = l & 3;
    for (int i = 0; i < nu; ++i) {
      const int u = u0 + i, mat = u >= 688, v = u - 688 * mat, ks = v / 43, wc = v - 43 * ks;
      const char* src = sm + hs + i * 2048 + (8 * g + q) * 64 + 8 * p;
#pragma unroll
      for (int f = 0; f < 2; ++f) {
        const u32x2 v0 = tr_read(src + f * 32), v1 = tr_read(src + f * 32 + 256);
        st16((u32x4){v0[0], v0[1], v1[0], v1[1]}, R, l * 16, OFF_WGU + ((2 * wc + f) * 16 + ks) * 2048 + mat * 1024);
      }
    }
    if (w == 3) {
      if (l < 2) *(u32x4*)(sm + hs + l * 16) = tail;
      asm volatile("s_waitcnt lgkmcnt(0)" ::: "memory");
      const u16* lr = (const u16*)(sm + hs + 2048);
      const u16* tr_ = (const u16*)(sm + hs);
      for (int idx = l; idx < 2 * DF; idx += 64) {
        const int P = idx >> 1, h = idx & 1;
        u32x4 o;
#pragma unroll
        for (int e = 0; e < 4; ++e) {
          const int k = (4 * h + e) * DF + P;
          const u16* pr = (e == 3 && k >= 2048) ? tr_ + (k - 2048) : lr + k;
          const int io = (e == 3 && k >= 2048) ? 8 : 2048;
          o[e] = (unsigned)pr[0] | ((unsigned)pr[io] << 16);
        }
        st16(o, R, P * 8192 + h * 16, OFF_WT + b * 32);
        if ((P & 255) == 0) {
          st16((u32x4){o[0], tag, o[1], tag}, R, (P >> 8) * 16384 + h * 32, OFF_WS0 + b * 64);
          st16((u32x4){o[2], tag, o[3], tag}, R, (P >> 8) * 16384 + h * 32 + 16, OFF_WS0 + b * 64);
        }
      }
    }
    if (mr_on) {
      float a[8];
#pragma unroll
      for (int i = 0; i < 8; ++i) {
        const float sc = silu_b((i & 1) ? cv[i >> 1] >> 16 : cv[i >> 1]);
#pragma unroll
        for (int m = 0; m < 4; ++m) {
          a[2 * m] = (i ? a[2 * m] : 0.f) + sc * lo(wm[i][m]);
          a[2 * m + 1] = (i ? a[2 * m + 1] : 0.f) + sc * hi(wm[i][m]);
        }
      }
#pragma unroll
      for (int e = 0; e < 8; ++e) a[e] = dpp_add<0x128>(dpp_add<0x124>(a[e]));
      if (c < 4) {
        ((f32x4*)(sm + L_RED2))[(t4 >> 4) * 8 + 2 * c] = (f32x4){a[0], a[1], a[2], a[3]};
        ((f32x4*)(sm + L_RED2))[(t4 >> 4) * 8 + 2 * c + 1] = (f32x4){a[4], a[5], a[6], a[7]};
      }
    }
    STAMP(4);
    BAR();
    if (mr_on && w < 2) {
      f32x4 s = ((const f32x4*)(sm + L_RED2))[c * 8 + 4 * w + g];
#pragma unroll
      for (int e = 0; e < 4; ++e) s[e] = row_sum(s[e]);
      if (c == 0) __builtin_amdgcn_raw_buffer_store_b64((u32x2){pk(s[0], s[1]), pk(s[2], s[3])}, R, (4 * w + g) * 8, OFF_MODR + mslice * 64, 16);
    }
    DRAIN();
    STAMP(5);
    BAR();
    if (wd_on) {
      const char* src = sm + hs + 4096 + (4 * g + q) * 64 + 16 * p;
#pragma unroll
      for (int f = 0; f < 2; ++f) {
        const u32x2 v0 = tr_read(src + f * 8), v1 = tr_read(src + f * 8 + 1024);
        st16((u32x4){v0[0], v0[1], v1[0], v1[1]}, R, l * 16, OFF_WD + ((((h3 & 15) >> 1) * 43 + (h3 >> 4)) * 4 + 2 * (h3 & 1) + f) * 1024);
      }
    }
    DRAIN();
    STAMP(6);
    BAR();
    STAMP(7);
#pragma unroll
    for (int ks = 0; ks < 6; ++ks) loadW(ks);
    STAMP(8);
    if (b == 0) BAR();
    BAR();
#pragma unroll
    for (int ks = 6; ks < CW3; ++ks) loadW(ks);
    STAMP(9);
    BAR();
    BAR();
  }

  {
    const int kg = tid >> 3, rr = tid & 7;
    st16(*(const u32x4*)(sm + L_H2 + kg * 128 + ((rr + kg) & 7) * 16), R, kg * 256 + rr * 16, OFF_H2 + (b >> 1) * 16384 + (b & 1) * 128);
  }
  DRAIN();
  BAR();
  if (tid == 0) st4(tag, R, 0, OFF_FD + b * 128);
  if (!crit) {
    st16(*(const u32x4*)(sm + L_X1 + t4 * 16), R, t4 * 16, OFF_X1 + b * 8192);
    st16(*(const u32x4*)(sm + L_X1 + 4096 + t4 * 16), R, t4 * 16 + 4096, OFF_X1 + b * 8192);
  } else {
#pragma unroll
    for (int ks = CW2; ks < CW3; ++ks) loadW(ks);
  }
  FENCE();
  STAMP(11);

  const int tg = xcd * 4 + (loc >> 3), dg = loc & 7;
  u32x4 wdf[6][4];
  auto loadD = [&](int i) __attribute__((always_inline)) {
    const unsigned base = OFF_WD + (dg * 43 + min(8 * i + w8, 42)) * 4096;
#pragma unroll
    for (int j = 0; j < 4; ++j) wdf[i][j] = ld16(R, l * 16 + j * 1024, base);
  };
  if (g1_on) {
    if (w8 == 0) wait_flags(R, min(12 * pm + l, 255) * 128, OFF_FD, tag, l < 12);
    BAR();
    STAMP(18);
    u32x4* As = (u32x4*)(sm + L_AS) + ((w8 >> 1) * 6 + 3 * (w8 & 1)) * 64 + l;
    const u32x4* Ar = (const u32x4*)(sm + L_AS) + l;
    u32x4 ar[4][3];
    unsigned abase[3];
#pragma unroll
    for (int i = 0; i < 3; ++i) abase[i] = OFF_H2 + min(6 * pm + 3 * (w8 & 1) + i, 127) * 16384 + (w8 >> 1) * 1024;
    auto loadA = [&](int qd) __attribute__((always_inline)) {
#pragma unroll
      for (int i = 0; i < 3; ++i) ar[qd][i] = ld16(R, l * 16, abase[i] + qd * 4096);
    };
    auto stageA = [&](int qd) __attribute__((always_inline)) {
#pragma unroll
      for (int i = 0; i < 3; ++i) As[(24 * qd + i) * 64] = ar[qd][i];
    };
    loadA(0); loadA(1);
    FENCE();
    f32x4 acc[2][6];
#pragma unroll
    for (int f = 0; f < 2; ++f)
#pragma unroll
      for (int tt = 0; tt < 6; ++tt) acc[f][tt] = z4;
    stageA(0);
    if (!crit && l == 0) st4(tag, R, 0, OFF_FX + b * 16 + w * 4);
    BAR();
    STAMP(12);
    u32x4 bq[96];
#pragma unroll
    for (int n = 0; n < PF; ++n) bq[n] = Ar[n * 64];
#pragma unroll
    for (int ks = 0; ks < 16; ++ks) {
      SB();
      if (ks == 0) loadA(2);
      if (ks == 2) loadA(3);
      if (CW3 + ks < 16) loadW(CW3 + ks);
      if (ks >= 4 && ks < 10) loadD(ks - 4);
      SB();
#pragma unroll
      for (int tt = 0; tt < 6; ++tt) {
        const int n = ks * 6 + tt;
        if (n % 24 == 24 - PF - 4 && n < 72) { stageA(n / 24 + 1); }
        if (n % 24 == 24 - PF && n < 72) { BAR(); }
        if (n + PF < 96) bq[n + PF] = Ar[(n + PF) * 64];
        acc[0][tt] = mfma(wf[ks][0], bq[n], acc[0][tt]);
        acc[1][tt] = mfma(wf[ks][1], bq[n], acc[1][tt]);
        SB();
      }
    }
    STAMP(13);
    if (ft < 86) {
#pragma unroll
      for (int pr = 0; pr < 3; ++pr) {
        u32x4 o;
#pragma unroll
        for (int q = 0; q < 2; ++q)
#pragma unroll
          for (int e2 = 0; e2 < 2; ++e2) {
            const unsigned gg = pk(acc[0][2 * pr + q][2 * e2], acc[0][2 * pr + q][2 * e2 + 1]);
            const unsigned uu = pk(acc[1][2 * pr + q][2 * e2], acc[1][2 * pr + q][2 * e2 + 1]);
            o[2 * q + e2] = pk(lo(gg) * lo(uu), hi(gg) * hi(uu));
          }
        if (3 * pm + pr < 64) st16(o, R, l * 16, OFF_GU + ((3 * pm + pr) * 86 + ft) * 1024);
      }
    }
    DRAIN();
    BAR();
    if (tid == 0) st4(tag, R, 0, OFF_FE + (pm * 11 + pn) * 128);
  } else {
#pragma unroll
    for (int i = 0; i < 6; ++i) loadD(i);
    DRAIN();
    if (!crit && l == 0) st4(tag, R, 0, OFF_FX + b * 16 + w * 4);
    BAR();
  }
  FENCE();
  STAMP(14);

  {
    const int tt_o = w8 >> 1, tp_o = w8 & 1;
    const int c3 = 2 * (4 * tg + tt_o) + (c >> 3);
    const int row = 32 * (4 * (c3 >> 5) + (c & 3)) + (c3 & 31) + 1024 * ((c >> 2) & 1);
    if (w8 == 0) {
      const int pmx = (l < 11 ? 4 * tg : 4 * tg + 3) / 6;
      const unsigned fo = l < 22 ? OFF_FE + (pmx * 11 + (l < 11 ? l : l - 11)) * 128 : OFF_FX + 8 * tg * 16 + ((l - 22) & 31) * 4;
      wait_flags(R, fo, 0, tag, l < 54);
    }
    BAR();
    STAMP(19);
    u32x4 gf[6][2][2];
    auto loadG = [&](int i) __attribute__((always_inline)) {
      const unsigned base = OFF_GU + (2 * tg * 86 + 2 * min(8 * i + w8, 42)) * 1024;
#pragma unroll
      for (int pr = 0; pr < 2; ++pr) {
        gf[i][pr][0] = ld16(R, l * 16, base + pr * 86 * 1024);
        gf[i][pr][1] = ld16(R, l * 16 + 1024, base + pr * 86 * 1024);
      }
    };
#pragma unroll
    for (int i = 0; i < GWIN; ++i) loadG(i);
    FENCE();
    f32x4 acc[4][4];
#pragma unroll
    for (int j = 0; j < 4; ++j)
#pragma unroll
      for (int tt = 0; tt < 4; ++tt) acc[j][tt] = z4;
    u32x4 xr, gbv;
#pragma unroll
    for (int i = 0; i < 6; ++i) {
      if (i < 5 || w8 < 3) {
#pragma unroll
        for (int tt = 0; tt < 4; ++tt) {
          const u32x4 lo4 = gf[i][tt >> 1][0], hi4 = gf[i][tt >> 1][1];
          const u32x4 bf = {lo4[2 * (tt & 1)], lo4[2 * (tt & 1) + 1], hi4[2 * (tt & 1)], hi4[2 * (tt & 1) + 1]};
#pragma unroll
          for (int j = 0; j < 4; ++j) acc[j][tt] = mfma(wdf[i][j], bf, acc[j][tt]);
        }
      }
      FENCE();
      if (i + GWIN < 6) loadG(i + GWIN);
      if (i == 6 - GWIN) {
        xr = ld16(R, c3 * 8192 + (c & 7) * 1024 + (64 * dg + 32 * tp_o + 8 * g) * 2, OFF_X1);
        gbv = ld16(R, (64 * dg + 32 * tp_o + 8 * g) * 2, OFF_MODR + 3072);
      }
      FENCE();
    }
    STAMP(15);
    f32x4* red = (f32x4*)sm + l;
#pragma unroll
    for (int tt = 0; tt < 4; ++tt)
#pragma unroll
      for (int j = 0; j < 4; ++j) red[((tt * 4 + j) * 8 + w8) * 64] = acc[j][tt];
    BAR();
    f32x4 mine[2] = {z4, z4};
#pragma unroll
    for (int s = 0; s < 8; ++s)
#pragma unroll
      for (int j = 0; j < 2; ++j) mine[j] += red[((2 * w8 + j) * 8 + s) * 64];
    u32x4 o;
#pragma unroll
    for (int m = 0; m < 4; ++m) {
      const f32x4 v = mine[m >> 1];
      const unsigned y = pk(v[2 * (m & 1)], v[2 * (m & 1) + 1]);
      const unsigned gy = pk(lo(gbv[m]) * lo(y), hi(gbv[m]) * hi(y));
      o[m] = pk(lo(xr[m]) + lo(gy), hi(xr[m]) + hi(gy));
    }
    *(u32x4*)(out + (size_t)row * D_ + 64 * dg + 32 * tp_o + 8 * g) = o;
  }
  STAMP(16);
  STAMP_FLUSH();
}

extern "C" void kernel_launch(void* const* d_in, const int* in_sizes, int n_in,
                              void* d_out, int out_size, void* d_ws, size_t ws_size,
                              hipStream_t stream) {
  (void)hipFuncSetAttribute((const void*)fused_block, hipFuncAttributeMaxDynamicSharedMemorySize, LDS_BYTES);
  fused_block<<<256, 512, LDS_BYTES, stream>>>((const u16*)d_in[0], (const u16*)d_in[1], (const u16*)d_in[2],
                                               (const u16*)d_in[4], (const u16*)d_in[5], (const u16*)d_in[6],
                                               (const u16*)d_in[7], (const u16*)d_in[8], (u16*)d_out, (char*)d_ws);
}
```

```cpp
#include <hip/hip_runtime.h>
#include <hip/hip_bf16.h>
#include <stdint.h>

typedef __attribute__((ext_vector_type(8))) __bf16 bf16x8;
typedef __attribute__((ext_vector_type(2))) __bf16 bf16x2;
typedef __attribute__((ext_vector_type(4))) short s16x4;
typedef __attribute__((ext_vector_type(4))) float f32x4;
typedef __attribute__((ext_vector_type(2))) float f32x2;
typedef __attribute__((ext_vector_type(4))) unsigned u32x4;
typedef __attribute__((ext_vector_type(2))) unsigned u32x2;
typedef unsigned short u16;
typedef __amdgpu_buffer_rsrc_t rsrc_t;

#define DI __device__ __forceinline__
#define LAS __attribute__((address_space(3)))

#define STAMP(slot)
#define STAMP_DECL
#define STAMP_FLUSH()

constexpr int D_ = 512, F_ = 1376, DF = 257;
constexpr unsigned SENT = 0xFFFFFFFFu;
constexpr unsigned SPIN_CAP = 1u << 15;

constexpr unsigned OFF_EP   = 0;
constexpr unsigned OFF_GA   = 4096;
constexpr unsigned OFF_GT   = 8192;
constexpr unsigned OFF_FX   = 12288;
constexpr unsigned OFF_MODT = 16384;
constexpr unsigned OFF_WS0  = 98304;
constexpr unsigned OFF_WT   = 131072;
constexpr unsigned OFF_WGU  = OFF_WT + 257u * 2048u * 4u;
constexpr unsigned OFF_WD   = OFF_WGU + 86u * 16u * 2u * 1024u;
constexpr unsigned OFF_T1   = OFF_WD + 8u * 43u * 4u * 1024u;
constexpr unsigned OFF_T2   = OFF_T1 + (2u << 20);
constexpr unsigned OFF_H2   = OFF_T2 + (2u << 20);
constexpr unsigned OFF_X1   = OFF_H2 + (2u << 20);
constexpr unsigned OFF_GU   = OFF_X1 + (2u << 20);
constexpr unsigned WS_BYTES = OFF_GU + 64u * 86u * 1024u;

constexpr int L_RED = 0, L_MODA = 2048, L_IMG1 = 4096, L_INB = 12544, L_EX = 20736;
constexpr int L_RED2 = 28928, L_CNT = 31744, L_WST = 32768;
constexpr int L_REV = L_IMG1;
constexpr int L_IMG3 = 0, L_H2 = 12544, L_X1 = 20736, L_AS = 32768;
constexpr int LDS_BYTES = 163840;

#define CODE_PF 1
#define HW0 2
#define HW1 8
constexpr int GWIN = 4, PF = 4;

DI unsigned pk(float a, float b) {
  f32x2 v = {a, b};
  return __builtin_bit_cast(unsigned, __builtin_convertvector(v, bf16x2));
}
DI float lo(unsigned u) { return __uint_as_float(u << 16); }
DI float hi(unsigned u) { return __uint_as_float(u & 0xffff0000u); }
DI float rb(float a) { return lo(pk(a, a)); }
DI f32x4 mfma(u32x4 a, u32x4 b, f32x4 c) {
  return __builtin_amdgcn_mfma_f32_16x16x32_bf16(__builtin_bit_cast(bf16x8, a), __builtin_bit_cast(bf16x8, b), c, 0, 0, 0);
}
DI float cosr(float rev) { return __builtin_amdgcn_cosf(rev); }
DI float sinr(float rev) { return __builtin_amdgcn_sinf(rev); }

DI u32x4 ld16(rsrc_t r, unsigned v, unsigned s) { return __builtin_amdgcn_raw_buffer_load_b128(r, v, s, 16); }
DI u32x2 ld8(rsrc_t r, unsigned v, unsigned s) { return __builtin_amdgcn_raw_buffer_load_b64(r, v, s, 16); }
DI unsigned ld4(rsrc_t r, unsigned v, unsigned s) { return __builtin_amdgcn_raw_buffer_load_b32(r, v, s, 16); }
DI void st16(u32x4 x, rsrc_t r, unsigned v, unsigned s) { __builtin_amdgcn_raw_buffer_store_b128(x, r, v + s, 0, 16); }
DI void st4(unsigned x, rsrc_t r, unsigned v, unsigned s) { __builtin_amdgcn_raw_buffer_store_b32(x, r, v, s, 16); }

#define FENCE() do { asm volatile("" ::: "memory"); __builtin_amdgcn_sched_barrier(0); } while (0)
#define BAR() do { asm volatile("s_waitcnt lgkmcnt(0)" ::: "memory"); __builtin_amdgcn_s_barrier(); FENCE(); } while (0)
#define DRAIN() asm volatile("s_waitcnt vmcnt(0)" ::: "memory")
#define SB() __builtin_amdgcn_sched_barrier(0)
#define PIN(x) asm volatile("" : "+v"(x))

DI void glds16(const void* src, unsigned lds_base) {
  unsigned keep;
  asm volatile("s_mov_b32 %0, m0\n\ts_mov_b32 m0, %2\n\ts_nop 0\n\tglobal_load_lds_dwordx4 %1, off\n\ts_mov_b32 m0, %0"
               : "=&s"(keep) : "v"(src), "s"(lds_base) : "memory");
}
DI unsigned umax3(unsigned a, unsigned b, unsigned c) { return max(max(a, b), c); }
DI unsigned vmax(u32x4 v, unsigned m) { return umax3(v[2], v[3], umax3(v[0], v[1], m)); }
DI bool wave_any(bool p) { return __builtin_amdgcn_ballot_w64(p) != 0ull; }

template <int CTRL> DI float dpp_add(float v) {
  int s = __builtin_amdgcn_update_dpp(0, __float_as_int(v), CTRL, 0xf, 0xf, true);
  return v + __int_as_float(s);
}
DI float row_sum(float v) { v = dpp_add<0xB1>(v); v = dpp_add<0x4E>(v); v = dpp_add<0x141>(v); return dpp_add<0x140>(v); }
DI float wave_sum(float v) {
  const int i = __float_as_int(row_sum(v));
  const float a = __int_as_float(__builtin_amdgcn_readlane(i, 0)), b = __int_as_float(__builtin_amdgcn_readlane(i, 16));
  const float c = __int_as_float(__builtin_amdgcn_readlane(i, 32)), d = __int_as_float(__builtin_amdgcn_readlane(i, 48));
  return (a + b) + (c + d);
}
DI void ln_stats(const float (&x)[8], float& mean_b, float& inv_sd) {
  float s = ((x[0] + x[1]) + (x[2] + x[3])) + ((x[4] + x[5]) + (x[6] + x[7]));
  const float mean = wave_sum(s) * (1.f / 512.f);
  float q0 = 0.f, q1 = 0.f;
#pragma unroll
  for (int i = 0; i < 4; ++i) { const float d = x[i] - mean, e = x[i + 4] - mean; q0 += d * d; q1 += e * e; }
  const float var = rb(wave_sum(q0 + q1) * (1.f / 512.f));
  const float sd = rb(__builtin_amdgcn_sqrtf(rb(var + 1e-5f)));
  mean_b = rb(mean);
  inv_sd = __builtin_amdgcn_rcpf(sd);
}
DI float silu_b(unsigned bits) {
  const float c = lo(bits);
  return rb(c * __builtin_amdgcn_rcpf(1.f + __expf(-c)));
}
DI u32x4 twiddle(f32x4 re, f32x4 im, f32x4 C, f32x4 S) {
  const f32x4 r = re * C + im * S, i = im * C - re * S;
  return (u32x4){pk(r[0], i[0]), pk(r[1], i[1]), pk(r[2], i[2]), pk(r[3], i[3])};
}
DI void dft_frag(float base, float step, u32x4& fre, u32x4& fim) {
#pragma unroll
  for (int e = 0; e < 4; ++e) {
    const float a = base + (float)e * step, cs = cosr(a), sn = sinr(a);
    fre[e] = pk(cs, sn); fim[e] = pk(-sn, cs);
  }
}
DI void tw_frag(float base, float step, f32x4& C, f32x4& S) {
#pragma unroll
  for (int e = 0; e < 4; ++e) { const float a = base + (float)e * step; C[e] = cosr(a); S[e] = sinr(a); }
}
DI u32x4 flip(u32x4 v, unsigned m) { return (u32x4){v[0] ^ m, v[1] ^ m, v[2] ^ m, v[3] ^ m}; }
DI u32x4 flip_odd(u32x4 v) { return (u32x4){v[0], v[1] ^ 0x80008000u, v[2], v[3] ^ 0x80008000u}; }
DI unsigned lds_arrive(char* p) {
  asm volatile("s_waitcnt lgkmcnt(0)" ::: "memory");
  unsigned old = 0;
  if ((threadIdx.x & 63) == 0) old = __hip_atomic_fetch_add((LAS unsigned*)p, 1u, __ATOMIC_RELAXED, __HIP_MEMORY_SCOPE_WORKGROUP);
  return __builtin_amdgcn_readfirstlane(old);
}
DI void lds_wait(char* p, unsigned want) {
  for (unsigned spins = 0; spins < (1u << 24); ++spins) {
    if (__builtin_amdgcn_readfirstlane(__hip_atomic_load((LAS unsigned*)p, __ATOMIC_RELAXED, __HIP_MEMORY_SCOPE_WORKGROUP)) >= want) break;
    __builtin_amdgcn_s_sleep(1);
  }
  FENCE();
}
DI u32x2 tr_read(const char* p) {
  return __builtin_bit_cast(u32x2, __builtin_amdgcn_ds_read_tr16_b64_v4i16((LAS s16x4*)p));
}

DI void wait_flags(rsrc_t R, unsigned voff, unsigned soff, unsigned tag, bool active) {
  for (unsigned spins = 0;; ++spins) {
    FENCE();
    const unsigned v = ld4(R, voff, soff);
    if (!wave_any(active && v != tag) || spins > SPIN_CAP) break;
    __builtin_amdgcn_s_sleep(2);
  }
  FENCE();
}

__global__ __launch_bounds__(512, 2) void fused_block(const u16* __restrict__ x, const u16* __restrict__ wr,
                                                      const u16* __restrict__ wi, const u16* __restrict__ wg,
                                                      const u16* __restrict__ wu, const u16* __restrict__ wd,
                                                      const u16* __restrict__ cvec, const u16* __restrict__ wmod,
                                                      u16* out, char* ws) {
  extern __shared__ __attribute__((aligned(16))) char sm[];
  const int b = blockIdx.x, tid = threadIdx.x, w8 = __builtin_amdgcn_readfirstlane(tid >> 6), l = tid & 63, c = l & 15, g = l >> 4;
  const int w = w8 & 3, t4 = tid & 255;
  const bool crit = w8 < 4;
  const rsrc_t R = __builtin_amdgcn_make_buffer_rsrc(ws, 0, WS_BYTES, 0x00020000);
  const rsrc_t RM = __builtin_amdgcn_make_buffer_rsrc((void*)wmod, 0, 512 * 3072 * 2, 0x00020000);
  const f32x4 z4 = {0.f, 0.f, 0.f, 0.f};
  const unsigned lds0 = (unsigned)(size_t)(LAS char*)sm;
  asm volatile(".Lfb_begin:");
  STAMP_DECL;
  STAMP(0);

  const int xcd = b & 7, loc = b >> 3;
  const bool g1_on = loc < 30 + (xcd < 2);
  const int T1i = g1_on ? xcd * 30 + (xcd < 2 ? xcd : 2) + loc : 0;
  const int hh = T1i >= 121, T1j = T1i - 121 * hh, pn = T1j / 11, pm = 11 * hh + (T1j - 11 * pn);
  const int ft = 8 * pn + w8;
  u32x4 wf[16][2];
  if (tid < 8) ((unsigned*)(sm + L_CNT))[tid] = 0u;
  const bool ga_on = b >= 8 && b < 72;
  const int slice = (b & 7) * 8 + ((b - 8) >> 3);
  unsigned tag;

  if (crit) {
    u32x4 wm[4];
    if (ga_on) {
#pragma unroll
      for (int i = 0; i < 4; ++i) wm[i] = __builtin_amdgcn_raw_buffer_load_b128(RM, (t4 >> 1) * 6144 + (t4 & 1) * 16, slice * 32 + i * (128 * 6144), 0);
    }
    unsigned cv[4];
#pragma unroll
    for (int i = 0; i < 4; ++i) cv[i] = cvec[(t4 >> 1) + 128 * i];
    FENCE();
    const int sp1 = 32 * (8 * ((b >> 5) & 3) + 2 * ((b >> 3) & 3) + (w >> 1)) + 16 * (b >> 7) + 2 * (b & 7) + (w & 1);
    unsigned ua[4], ub[4];
    {
      const unsigned* xa = (const unsigned*)(x + (size_t)sp1 * D_) + 64 * g + c;
#pragma unroll
      for (int e = 0; e < 4; ++e) { ua[e] = xa[16 * e]; ub[e] = xa[16 * e + 1024 * D_ / 2]; }
    }
    BAR();
    const float fc = (float)c, fg4 = (float)(4 * g), fj1 = (float)(16 * w + c);
    u32x4 k16[2], k32a[2][2][2], k32[2][2][2];
    f32x4 tw1c[2], tw1s[2], t1c[2], t1s[2];
    const unsigned codd = (c & 1) ? 0x80008000u : 0u;
    dft_frag(fg4 * fc * (1.f / 16.f), fc * (1.f / 16.f), k16[0], k16[1]);
    dft_frag((2.f * fg4 + 1.f) * fc * (1.f / 32.f), fc * (1.f / 16.f), k32a[0][0][1], k32a[0][1][1]);
#pragma unroll
    for (int pt = 0; pt < 2; ++pt) {
      k32a[1][pt][1] = flip(k32a[0][pt][1], 0x80008000u);
      PIN(k32a[0][pt][1]); PIN(k32a[1][pt][1]);
    }
#pragma unroll
    for (int t = 0; t < 2; ++t) {
      tw_frag((2.f * fg4 + t) * fc * (1.f / 512.f), fc * (1.f / 256.f), tw1c[t], tw1s[t]);
      PIN(tw1c[t]); PIN(tw1s[t]);
    }
    PIN(k16[0]); PIN(k16[1]);
    float sc[4];
#pragma unroll
    for (int i = 0; i < 4; ++i) { sc[i] = silu_b(cv[i]); PIN(sc[i]); }
    lds_wait(sm + L_CNT + 8, 1u);
    tag = __builtin_amdgcn_readfirstlane(*(const unsigned*)(sm + L_CNT + 4));
    FENCE();
    STAMP(1);
    if (ga_on) {
      float a[8];
#pragma unroll
      for (int i = 0; i < 4; ++i) {
#pragma unroll
        for (int m = 0; m < 4; ++m) {
          a[2 * m] = (i ? a[2 * m] : 0.f) + sc[i] * lo(wm[i][m]);
          a[2 * m + 1] = (i ? a[2 * m + 1] : 0.f) + sc[i] * hi(wm[i][m]);
        }
      }
#pragma unroll
      for (int e = 0; e < 8; ++e) a[e] = dpp_add<0x128>(dpp_add<0x124>(dpp_add<0x122>(a[e])));
      if (c < 2) {
        ((f32x4*)(sm + L_RED))[(t4 >> 4) * 4 + 2 * c] = (f32x4){a[0], a[1], a[2], a[3]};
        ((f32x4*)(sm + L_RED))[(t4 >> 4) * 4 + 2 * c + 1] = (f32x4){a[4], a[5], a[6], a[7]};
      }
      if (lds_arrive(sm + L_CNT) == 3u) {
        f32x4 s = ((const f32x4*)(sm + L_RED))[c * 4 + g];
#pragma unroll
        for (int e = 0; e < 4; ++e) s[e] = row_sum(s[e]);
        if (c == 0) st16((u32x4){pk(s[0], s[1]), tag, pk(s[2], s[3]), tag}, R, g * 16, OFF_GA + slice * 64);
        if (l == 0) *(volatile unsigned*)(sm + L_CNT + 24) = 1u;
      }
    }
    FENCE();
    STAMP(2);
    float na[8], nb[8];
    {
#pragma unroll
      for (int e = 0; e < 4; ++e) { na[2 * e] = lo(ua[e]); na[2 * e + 1] = hi(ua[e]); nb[2 * e] = lo(ub[e]); nb[2 * e + 1] = hi(ub[e]); }
      float ma, ia, mb, ib;
      ln_stats(na, ma, ia);
      ln_stats(nb, mb, ib);
#pragma unroll
      for (int e = 0; e < 8; ++e) { na[e] = (na[e] - ma) * ia; nb[e] = (nb[e] - mb) * ib; PIN(na[e]); PIN(nb[e]); }
    }
    dft_frag(fg4 * fc * (1.f / 32.f), fc * (1.f / 32.f), k32[0][0][0], k32[0][1][0]);
#pragma unroll
    for (int pt = 0; pt < 2; ++pt) {
      k32[0][pt][1] = flip(k32[0][pt][0], codd);
      k32[1][pt][0] = flip_odd(k32[0][pt][0]);
      k32[1][pt][1] = flip_odd(k32[0][pt][1]);
      PIN(k32[0][pt][0]); PIN(k32[0][pt][1]); PIN(k32[1][pt][0]); PIN(k32[1][pt][1]);
    }
    u32x4 k1c[4][2], k4c[4][2];
    f32x4 t3c[2], t3s[2];
    auto gen_p2 = [&]() __attribute__((always_inline)) {
    {
#pragma unroll
      for (int t = 0; t < 2; ++t) {
        tw_frag((fg4 + 16.f * t) * fj1 * (1.f / 2048.f), fj1 * (1.f / 2048.f), t1c[t], t1s[t]);
        PIN(t1c[t]); PIN(t1s[t]);
      }
      const float sg = (c & 1) ? -1.f : 1.f;
      float gr[8], gi[8];
#pragma unroll
      for (int i = 0; i < 8; ++i) {
        const float a = (0.5f * fg4 + (float)(8 * (i >> 1) + (i & 1))) * (fj1 * (1.f / 64.f));
        gr[i] = cosr(a); gi[i] = -sinr(a);
      }
      if (b != 0) {
#pragma unroll
        for (int i = 0; i < 8; ++i) {
          const float A = 0.5f * (gr[i] + sg * gi[i]), B = 0.5f * (gr[i] - sg * gi[i]);
          k1c[i >> 1][0][i & 1] = pk(A, sg * B); k1c[i >> 1][0][2 + (i & 1)] = pk(B, sg * A);
          k1c[i >> 1][1][i & 1] = pk(-sg * B, A); k1c[i >> 1][1][2 + (i & 1)] = pk(sg * A, -B);
        }
      } else {
#pragma unroll
        for (int i = 0; i < 8; ++i) {
          k1c[i >> 1][0][i & 1] = pk(gr[i], sg * gr[i]); k1c[i >> 1][0][2 + (i & 1)] = pk(-gi[i], -sg * gi[i]);
          k1c[i >> 1][1][i & 1] = pk(gi[i], sg * gi[i]); k1c[i >> 1][1][2 + (i & 1)] = pk(gr[i], sg * gr[i]);
        }
      }
#pragma unroll
      for (int i = 0; i < 8; ++i) PIN(k1c[i >> 1][i & 1]);
    }
    {
#pragma unroll
      for (int t = 0; t < 2; ++t) {
        const float v = (fc + 16.f * t) * (1.f / 2048.f);
        tw_frag(((float)(16 * w) + fg4) * v, v, t3c[t], t3s[t]);
        PIN(t3c[t]); PIN(t3s[t]);
      }
      const int n1p = 8 * w + (c & 7);
      const bool col1 = c >= 8;
      float gr[8], gi[8];
#pragma unroll
      for (int i = 0; i < 8; ++i) {
        const float a = (fg4 + (float)(16 * (i >> 2) + (i & 3))) * ((float)n1p * (1.f / 64.f));
        gr[i] = cosr(a); gi[i] = -sinr(a);
      }
      const float cs1 = col1 ? -1.f : 1.f;
      if (b != 0) {
#pragma unroll
        for (int i = 0; i < 8; ++i) {
          const float s = (i & 1) ? -cs1 : cs1;
          const float U = gr[i] + s * gi[i], V = gr[i] - s * gi[i];
          k4c[i >> 2][0][i & 3] = pk(U, s * V);
          k4c[i >> 2][1][i & 3] = pk(-cs1 * s * V, cs1 * U);
        }
      } else {
#pragma unroll
        for (int i = 0; i < 8; ++i) {
          const float s = (i & 1) ? -1.f : 1.f;
          const float U = col1 ? -gi[i] : gr[i], V = col1 ? -gr[i] : -gi[i];
          k4c[i >> 2][0][i & 3] = pk(U, V);
          k4c[i >> 2][1][i & 3] = pk(-s * U, -s * V);
        }
      }
#pragma unroll
      for (int i = 0; i < 4; ++i) k4c[2 + (i >> 1)][i & 1] = flip(k4c[i >> 1][i & 1], codd);
#pragma unroll
      for (int i = 0; i < 8; ++i) PIN(k4c[i >> 1][i & 1]);
    }
    };
    if (!ga_on) gen_p2();
    STAMP(3);
    {
      u32x4 v;
      for (unsigned spins = 0;; ++spins) {
        FENCE();
        v = ld16(R, t4 * 16, OFF_GA);
        const unsigned t = ld4(R, t4 * 16, OFF_GT);
        if (!wave_any(v[1] != tag || v[3] != tag || (t != tag && t4 < 192)) || spins > SPIN_CAP) break;
        __builtin_amdgcn_s_sleep(6);
      }
      *(u32x2*)(sm + L_MODA + t4 * 8) = (u32x2){v[0], v[2]};
      BAR();
    }
    if (tid == 0 && b == 0) st4(tag, R, 0, OFF_EP);
    STAMP(4);

    {
      const unsigned* moda = (const unsigned*)(sm + L_MODA) + 64 * g + c;
      u32x4 af[2];
#pragma unroll
      for (int e = 0; e < 4; ++e) {
        const unsigned us = moda[16 * e], uc = moda[256 + 16 * e];
#pragma unroll
        for (int t = 0; t < 2; ++t) {
          const float ca1 = 1.f + (t ? hi(uc) : lo(uc)), sa = t ? hi(us) : lo(us);
          af[t][e] = pk(na[2 * e + t] * ca1 + sa, nb[2 * e + t] * ca1 + sa);
        }
      }
      u32x4 p1[2];
#pragma unroll
      for (int t = 0; t < 2; ++t) p1[t] = twiddle(mfma(af[t], k16[0], z4), mfma(af[t], k16[1], z4), tw1c[t], tw1s[t]);
      unsigned* img = (unsigned*)(sm + L_IMG1);
      const f32x4 re0 = mfma(k16[0], p1[0], z4), im0 = mfma(k16[1], p1[0], z4);
#pragma unroll
      for (int tp = 0; tp < 2; ++tp) {
        const f32x4 re = mfma(k32a[tp][0][1], p1[1], re0), im = mfma(k32a[tp][1][1], p1[1], im0);
#pragma unroll
        for (int e = 0; e < 4; ++e) {
          const int kk = c + 64 * g + 16 * e;
          const int p = tp ? ((256 - kk) & 255) : kk;
          img[(4 * (w & 1) + 2 * tp + (w >> 1)) * 260 + p] = pk(re[e], im[e]);
        }
      }
      BAR();
#pragma unroll
      for (int rep = 0; rep < 2; ++rep) {
        const int p = (t4 >> 1) + 128 * rep, qq = t4 & 1;
        u32x4 o;
#pragma unroll
        for (int e = 0; e < 4; ++e) o[e] = img[(4 * qq + e) * 260 + p];
        st16(o, R, p * 8192 + qq * 16, OFF_T1 + b * 32);
      }
    }
    FENCE();
    STAMP(5);
    if (ga_on) gen_p2();
    const int j0 = 16 * w + c + 256 * g;
    const float nrm = 1.f / (2048.f * 512.f);
    DRAIN();
    STAMP(6);
    unsigned wv[8];
    {
      u32x4 v[2];
      for (unsigned spins = 0;; ++spins) {
        FENCE();
        v[0] = ld16(R, t4 * 16, OFF_T1 + b * 8192);
        v[1] = ld16(R, t4 * 16 + 4096, OFF_T1 + b * 8192);
        if (!wave_any(vmax(v[0], vmax(v[1], 0u)) == SENT) || spins > SPIN_CAP) break;
        __builtin_amdgcn_s_sleep(2);
      }
      FENCE();
#pragma unroll
      for (int i = 0; i < 8; ++i) wv[i] = ld4(R, j0 * 4 + (i & 3) * 256, OFF_WT + b * 8192 + (i >> 2) * 4096);
      ((u32x4*)(sm + L_INB))[t4] = v[0];
      ((u32x4*)(sm + L_INB))[t4 + 256] = v[1];
    }
    STAMP(17);
    BAR();
    STAMP(7);

    {
      const u32x4* inb = (const u32x4*)(sm + L_INB) + l;
      u32x4 p1[2], p2[2];
#pragma unroll
      for (int t = 0; t < 2; ++t) {
        f32x4 re = z4, im = z4;
#pragma unroll
        for (int ks = 0; ks < 4; ++ks) {
          const u32x4 d = inb[(t * 4 + ks) * 64];
          re = mfma(d, k1c[ks][0], re); im = mfma(d, k1c[ks][1], im);
        }
        p1[t] = twiddle(re, im, t1c[t], t1s[t]);
      }
      f32x4 xr[2], xi[2];
#pragma unroll
      for (int tp = 0; tp < 2; ++tp) {
        xr[tp] = mfma(k32[tp][0][0], p1[0], z4); xi[tp] = mfma(k32[tp][1][0], p1[0], z4);
        xr[tp] = mfma(k32[tp][0][1], p1[1], xr[tp]); xi[tp] = mfma(k32[tp][1][1], p1[1], xi[tp]);
      }
      if (b != 0) {
#pragma unroll
        for (int tp = 0; tp < 2; ++tp) {
          f32x4 a, bb;
#pragma unroll
          for (int e = 0; e < 4; ++e) { a[e] = lo(wv[4 * tp + e]); bb[e] = hi(wv[4 * tp + e]); }
          a *= nrm; bb *= -nrm;
          const f32x4 yr = xr[tp] * a + xi[tp] * bb, yi = xr[tp] * bb - xi[tp] * a;
#pragma unroll
          for (int e = 0; e < 4; ++e) p2[tp][e] = pk(yr[e], yi[e]);
        }
      } else {
        unsigned* rev = (unsigned*)(sm + L_REV);
#pragma unroll
        for (int tp = 0; tp < 2; ++tp)
#pragma unroll
          for (int e = 0; e < 4; ++e) rev[j0 + 64 * (16 * tp + e)] = pk(xr[tp][e], xi[tp][e]);
        const f32x4* wq = (const f32x4*)(sm + L_WST + w * 8192) + l;
        f32x4 wpr[2], wpi[2], wmr[2], wmi[2];
#pragma unroll
        for (int tp = 0; tp < 2; ++tp) { wpr[tp] = wq[(4 * tp) * 64]; wpi[tp] = wq[(4 * tp + 1) * 64]; wmr[tp] = wq[(4 * tp + 2) * 64]; wmi[tp] = wq[(4 * tp + 3) * 64]; }
        BAR();
#pragma unroll
        for (int tp = 0; tp < 2; ++tp) {
          f32x4 nr, ni;
#pragma unroll
          for (int e = 0; e < 4; ++e) { const unsigned n = rev[(2048 - (j0 + 64 * (16 * tp + e))) & 2047]; nr[e] = lo(n); ni[e] = hi(n); }
          const f32x4 yr = xr[tp] * wpr[tp] - xi[tp] * wpi[tp] + nr * wmr[tp] + ni * wmi[tp];
          const f32x4 yi = ni * wmr[tp] - nr * wmi[tp] - xr[tp] * wpi[tp] - xi[tp] * wpr[tp];
#pragma unroll
          for (int e = 0; e < 4; ++e) p2[tp][e] = pk(yr[e], yi[e]);
        }
      }
      u32x4* ex = (u32x4*)(sm + L_EX);
#pragma unroll
      for (int t = 0; t < 2; ++t) {
        f32x4 re = mfma(p2[0], k32[t][0][0], z4), im = mfma(p2[0], k32[t][1][0], z4);
        re = mfma(p2[1], k32[t][0][1], re); im = mfma(p2[1], k32[t][1][1], im);
        ex[(w * 2 + t) * 64 + l] = twiddle(re, im, t3c[t], t3s[t]);
      }
      BAR();
#pragma unroll
      for (int t = 0; t < 2; ++t) {
        f32x4 re = z4, im = z4;
#pragma unroll
        for (int ks = 0; ks < 4; ++ks) {
          const u32x4 d = ex[(ks * 2 + t) * 64 + l];
          re = mfma(k4c[ks][0], d, re); im = mfma(k4c[ks][1], d, im);
        }
        u32x4 o;
#pragma unroll
        for (int e = 0; e < 4; ++e) o[e] = pk(re[e], im[e]);
        st16(o, R, ((2 * w + (g & 1)) * 32 + 16 * t + c) * 8192 + (g >> 1) * 16, OFF_T2 + b * 32);
      }
    }
    FENCE();
    STAMP(8);
    const int sp3 = 32 * (4 * (b >> 5) + w) + (b & 31);
    u32x2 xv[2][2], gav[2], sbv[2], cbv[2];
#pragma unroll
    for (int tp = 0; tp < 2; ++tp) {
      const int d0 = 256 * tp + 16 * c + 4 * g;
      xv[0][tp] = *(const u32x2*)(x + (size_t)sp3 * D_ + d0);
      xv[1][tp] = *(const u32x2*)(x + (size_t)(sp3 + 1024) * D_ + d0);
    }
    FENCE();
    {
      u32x4 mq[3][2];
      for (unsigned spins = 0;; ++spins) {
        FENCE();
        bool bad = false;
#pragma unroll
        for (int i = 0; i < 6; ++i) {
          mq[i >> 1][i & 1] = ld16(R, (128 * (i & 1) + 8 * c + 2 * g) * 8, OFF_MODT + (i >> 1) * 2048);
          bad |= mq[i >> 1][i & 1][1] != tag || mq[i >> 1][i & 1][3] != tag;
        }
        if (!wave_any(bad) || spins > SPIN_CAP) break;
        __builtin_amdgcn_s_sleep(4);
      }
#pragma unroll
      for (int tp = 0; tp < 2; ++tp) {
        gav[tp] = (u32x2){mq[0][tp][0], mq[0][tp][2]}; sbv[tp] = (u32x2){mq[1][tp][0], mq[1][tp][2]}; cbv[tp] = (u32x2){mq[2][tp][0], mq[2][tp][2]};
      }
    }
    f32x4 tw3c[2], tw3s[2];
#pragma unroll
    for (int t = 0; t < 2; ++t) {
      tw_frag((fg4 + 16.f * t) * fc * (1.f / 512.f), fc * (1.f / 512.f), tw3c[t], tw3s[t]);
      PIN(tw3c[t]); PIN(tw3s[t]);
    }
    DRAIN();
    STAMP(9);
    {
      u32x4 v[2];
      for (unsigned spins = 0;; ++spins) {
        FENCE();
        v[0] = ld16(R, t4 * 16, OFF_T2 + b * 8192);
        v[1] = ld16(R, t4 * 16 + 4096, OFF_T2 + b * 8192);
        if (!wave_any(vmax(v[0], vmax(v[1], 0u)) == SENT) || spins > SPIN_CAP) break;
        __builtin_amdgcn_s_sleep(2);
      }
      unsigned* img = (unsigned*)(sm + L_IMG3);
#pragma unroll
      for (int rep = 0; rep < 2; ++rep) {
        const int p = (t4 >> 1) + 128 * rep;
        const int k = (t4 & 1) ? (p ? 512 - p : 256) : p;
        const int pos = (k >> 5) * 36 + (k & 31);
#pragma unroll
        for (int e = 0; e < 4; ++e) img[e * 580 + pos] = v[rep][e];
      }
    }
    BAR();
    STAMP(10);

    {
      const unsigned* img = (const unsigned*)(sm + L_IMG3) + w * 580 + 4 * g * 36 + c;
      u32x4 p1[2];
#pragma unroll
      for (int t = 0; t < 2; ++t) {
        u32x4 af;
#pragma unroll
        for (int e = 0; e < 4; ++e) af[e] = img[e * 36 + 16 * t];
        p1[t] = twiddle(mfma(af, k16[0], z4), mfma(af, k16[1], z4), tw3c[t], tw3s[t]);
      }
      float x1[2][8];
#pragma unroll
      for (int tp = 0; tp < 2; ++tp) {
        f32x4 re = mfma(p1[0], k32[tp][0][0], z4), im = mfma(p1[0], k32[tp][1][0], z4);
        re = mfma(p1[1], k32[tp][0][1], re); im = mfma(p1[1], k32[tp][1][1], im);
#pragma unroll
        for (int e = 0; e < 4; ++e) {
          const float ga = (e & 1) ? hi(gav[tp][e >> 1]) : lo(gav[tp][e >> 1]);
          const float xa = (e & 1) ? hi(xv[0][tp][e >> 1]) : lo(xv[0][tp][e >> 1]);
          const float xb = (e & 1) ? hi(xv[1][tp][e >> 1]) : lo(xv[1][tp][e >> 1]);
          x1[0][4 * tp + e] = xa + ga * re[e];
          x1[1][4 * tp + e] = xb - ga * im[e];
        }
      }
      float mean[2], isd[2];
      ln_stats(x1[0], mean[0], isd[0]);
      ln_stats(x1[1], mean[1], isd[1]);
#pragma unroll
      for (int half = 0; half < 2; ++half) {
        const float m = mean[half], iv = isd[half];
        const int rr = 4 * half + w;
#pragma unroll
        for (int tp = 0; tp < 2; ++tp) {
          u32x2 xo, ho;
#pragma unroll
          for (int e2 = 0; e2 < 2; ++e2) {
            const float v0 = x1[half][4 * tp + 2 * e2], v1 = x1[half][4 * tp + 2 * e2 + 1];
            xo[e2] = pk(v0, v1);
            ho[e2] = pk((v0 - m) * iv * (1.f + lo(cbv[tp][e2])) + lo(sbv[tp][e2]),
                        (v1 - m) * iv * (1.f + hi(cbv[tp][e2])) + hi(sbv[tp][e2]));
          }
          *(u32x2*)(sm + L_X1 + rr * 1024 + (256 * tp + 16 * c + 4 * g) * 2) = xo;
          const int kg = 4 * (8 * tp + (c >> 1)) + 2 * (c & 1) + (g >> 1);
          *(u32x2*)(sm + L_H2 + kg * 128 + ((rr + kg) & 7) * 16 + (g & 1) * 8) = ho;
        }
      }
    }
    BAR();
#pragma unroll
    for (int rep = 0; rep < 2; ++rep) {
      const int kg = (t4 >> 3) + 32 * rep, rr = t4 & 7;
      st16(*(const u32x4*)(sm + L_H2 + kg * 128 + ((rr + kg) & 7) * 16), R, kg * 256 + rr * 16, OFF_H2 + (b >> 1) * 16384 + (b & 1) * 128);
    }
    FENCE();
    const u32x4* wst = (const u32x4*)(sm + L_WST + w * 32768) + l;
#pragma unroll
    for (int i = 0; i < 32; ++i) wf[i >> 1][i & 1] = wst[i * 64];
#pragma unroll
    for (int i = 0; i < 32; ++i) PIN(wf[i >> 1][i & 1]);
    DRAIN();
  } else {
    const u32x4 SENT4 = {SENT, SENT, SENT, SENT};
    unsigned ep_v = 0;
    if (w == 0) ep_v = ld4(R, 0, OFF_EP);
    FENCE();
    const int nb_ = b < 8 ? b : b - 64;
    if (!ga_on) {
#pragma unroll
      for (int i = 0; i < 8; ++i) st16(SENT4, R, t4 * 16 + i * 4096, OFF_T1 + nb_ * 32768);
    }
    FENCE();
    BAR();
    if (w == 0 && l == 0) { *(unsigned*)(sm + L_CNT + 4) = ep_v + 1u; *(volatile unsigned*)(sm + L_CNT + 8) = 1u; }
    if (w == 2) {
      unsigned long long pc; unsigned ofs, len;
      asm volatile("s_getpc_b64 %0\n.Lfb_here:\n\ts_mov_b32 %1, .Lfb_here-.Lfb_begin\n\ts_mov_b32 %2, .Lfb_end-.Lfb_begin" : "=s"(pc), "=s"(ofs), "=s"(len));
      const char* code = (const char*)((pc - ofs) & ~15ull);
      glds16(code + min((unsigned)(loc * 1024 + l * 16), (len - 16u) & ~15u), lds0 + L_WST + 49152);
      glds16(code + min((unsigned)(32768 + loc * 1024 + l * 16), (len - 16u) & ~15u), lds0 + L_WST + 49152);
    }
    const int hw = nb_ * 4 + w, h3 = hw / 3, hr = hw - 3 * h3;
    const bool wt_on = !ga_on && hr == 0;
    const int u0 = hr == 0 ? 2048 + h3 : 8 * h3 + 4 * (hr - 1);
    const int nu = ga_on ? 0 : (hr == 0 ? (h3 < 16 ? 1 : 0) : 4);
    const unsigned hs = L_WST + w * 12288;
    if (wt_on) {
      glds16((const u32x4*)(((l & 1) ? wi : wr) + (size_t)8 * h3 * DF) + 256, lds0 + hs + 10240);
#pragma unroll
      for (int i = 0; i < 8; ++i) glds16((const u32x4*)(((i & 4) ? wi : wr) + (size_t)8 * h3 * DF) + (i & 3) * 64 + l, lds0 + hs + 2048 + i * 1024);
    }
    for (int i = 0; i < nu; ++i) {
      const int u = u0 + i;
      const u16* src; int pitch;
      if (u < 1376) {
        const int mat = u >= 688, v = u - 688 * mat, ks = v / 43, wc = v - 43 * ks;
        src = (mat ? wu : wg) + (size_t)(32 * ks) * F_ + 32 * wc; pitch = F_;
      } else {
        const int v = u - 1376;
        src = wd + (size_t)(32 * (v >> 4)) * D_ + 32 * (v & 15); pitch = D_;
      }
      src += (size_t)(l >> 2) * pitch + 8 * (l & 3);
      glds16(src, lds0 + hs + i * 2048);
      glds16(src + (size_t)16 * pitch, lds0 + hs + i * 2048 + 1024);
    }
    STAMP(1);
    lds_wait(sm + L_CNT + 8, 1u);
    tag = __builtin_amdgcn_readfirstlane(*(const unsigned*)(sm + L_CNT + 4));
    asm volatile("s_waitcnt vmcnt(8)" ::: "memory");
    if (!ga_on && lds_arrive(sm + L_CNT + 16) == 3u && l == 0) st4(tag, R, 0, OFF_GT + nb_ * 16);
    STAMP(4);
    u32x4 wm[8];
    unsigned cv[8];
    if (ga_on) {
      lds_wait(sm + L_CNT + 24, 1u);
#pragma unroll
      for (int i = 0; i < 8; ++i) {
        wm[i] = __builtin_amdgcn_raw_buffer_load_b128(RM, (t4 >> 2) * 6144 + (l & 3) * 16, 2048 + slice * 64 + i * (64 * 6144), 0);
        cv[i] = cvec[(t4 >> 2) + 64 * i];
      }
    } else {
      DRAIN();
    }
    STAMP(2);
    const int q = c >> 2, p = l & 3;
    for (int i = 0; i < nu; ++i) {
      const int u = u0 + i;
      unsigned dst, fst; int base, ts, fs;
      if (u < 1376) {
        const int mat = u >= 688, v = u - 688 * mat, ks = v / 43, wc = v - 43 * ks;
        dst = OFF_WGU + (2 * wc * 16 + ks) * 2048 + mat * 1024; fst = 32768;
        base = (8 * g + q) * 64 + 8 * p; ts = 256; fs = 32;
      } else {
        const int v = u - 1376, ks = v >> 4, dq = v & 15;
        dst = OFF_WD + (((dq >> 1) * 43 + ks) * 4 + 2 * (dq & 1)) * 1024; fst = 1024;
        base = (4 * g + q) * 64 + 16 * p; ts = 1024; fs = 8;
      }
      const char* src = sm + hs + i * 2048 + base;
#pragma unroll
      for (int f = 0; f < 2; ++f) {
        const u32x2 v0 = tr_read(src + f * fs), v1 = tr_read(src + f * fs + ts);
        st16((u32x4){v0[0], v0[1], v1[0], v1[1]}, R, l * 16, dst + f * fst);
      }
    }
    if (wt_on) {
      const u16* lr = (const u16*)(sm + hs + 2048);
      const u16* tr_ = (const u16*)(sm + hs + 10240);
      for (int idx = l; idx < 2 * DF; idx += 64) {
        const int P = idx >> 1, h = idx & 1;
        u32x4 o;
#pragma unroll
        for (int e = 0; e < 4; ++e) {
          const int k = (4 * h + e) * DF + P;
          const u16* pr = (e == 3 && k >= 2048) ? tr_ + (k - 2048) : lr + k;
          const int io = (e == 3 && k >= 2048) ? 8 : 2048;
          o[e] = (unsigned)pr[0] | ((unsigned)pr[io] << 16);
        }
        st16(o, R, P * 8192 + h * 16, OFF_WT + h3 * 32);
        if ((P & 255) == 0) {
          st16((u32x4){o[0], tag, o[1], tag}, R, (P >> 8) * 16384 + h * 32, OFF_WS0 + h3 * 64);
          st16((u32x4){o[2], tag, o[3], tag}, R, (P >> 8) * 16384 + h * 32 + 16, OFF_WS0 + h3 * 64);
        }
      }
    }
    STAMP(3);
    BAR();
    if (!ga_on) DRAIN();
    STAMP(5);
    BAR();
    if (!ga_on) {
#pragma unroll
      for (int i = 0; i < 7; ++i) st16(SENT4, R, t4 * 16 + i * 4096, OFF_GU + nb_ * 29360);
      if (t4 < 43) st16(SENT4, R, t4 * 16 + 7 * 4096, OFF_GU + nb_ * 29360);
      DRAIN();
    }
    if (ga_on) {
      float a[8];
#pragma unroll
      for (int i = 0; i < 8; ++i) {
        const float sc = silu_b(cv[i]);
#pragma unroll
        for (int m = 0; m < 4; ++m) {
          a[2 * m] = (i ? a[2 * m] : 0.f) + sc * lo(wm[i][m]);
          a[2 * m + 1] = (i ? a[2 * m + 1] : 0.f) + sc * hi(wm[i][m]);
        }
      }
#pragma unroll
      for (int e = 0; e < 8; ++e) a[e] = dpp_add<0x128>(dpp_add<0x124>(a[e]));
      if (c < 4) {
        ((f32x4*)(sm + L_RED2))[(t4 >> 4) * 8 + 2 * c] = (f32x4){a[0], a[1], a[2], a[3]};
        ((f32x4*)(sm + L_RED2))[(t4 >> 4) * 8 + 2 * c + 1] = (f32x4){a[4], a[5], a[6], a[7]};
      }
      if (lds_arrive(sm + L_CNT + 20) == 3u) {
#pragma unroll
        for (int h = 0; h < 2; ++h) {
          f32x4 s = ((const f32x4*)(sm + L_RED2))[c * 8 + 4 * h + g];
#pragma unroll
          for (int e = 0; e < 4; ++e) s[e] = row_sum(s[e]);
          if (c == 0) st16((u32x4){pk(s[0], s[1]), tag, pk(s[2], s[3]), tag}, R, (4 * h + g) * 16, OFF_MODT + slice * 128);
        }
      }
    }
    const int j0 = 16 * w + c + 256 * g;
    const float nrm = 1.f / (2048.f * 512.f);
    if (b == 0) {
      f32x4 wpr[2], wpi[2], wmr[2], wmi[2];
      u32x2 q[2][4][4];
      for (unsigned spins = 0;; ++spins) {
        FENCE();
        bool bad = false;
#pragma unroll
        for (int i = 0; i < 8; ++i) {
          const int j = j0 + 64 * (16 * (i >> 2) + (i & 3)), jn = (2048 - j) & 2047;
          q[i >> 2][i & 3][0] = ld8(R, j * 8, OFF_WS0); q[i >> 2][i & 3][1] = ld8(R, jn * 8, OFF_WS0);
          q[i >> 2][i & 3][2] = ld8(R, j * 8, OFF_WS0 + 16384); q[i >> 2][i & 3][3] = ld8(R, jn * 8, OFF_WS0 + 16384);
        }
#pragma unroll
        for (int i = 0; i < 32; ++i) bad |= q[i >> 4][(i >> 2) & 3][i & 3][1] != tag;
        if (!wave_any(bad) || spins > SPIN_CAP) break;
        __builtin_amdgcn_s_sleep(8);
      }
#pragma unroll
      for (int i = 0; i < 8; ++i) {
        const unsigned a0 = q[i >> 2][i & 3][0][0], a1 = q[i >> 2][i & 3][1][0], b0 = q[i >> 2][i & 3][2][0], b1 = q[i >> 2][i & 3][3][0];
        const float sc4 = 0.25f * nrm;
        const float ar = (lo(a0) + lo(a1)) * sc4, ai = (hi(a0) - hi(a1)) * sc4;
        const float br = (lo(b0) + lo(b1)) * sc4, bi = (hi(b0) - hi(b1)) * sc4;
        wpr[i >> 2][i & 3] = ar + br; wpi[i >> 2][i & 3] = ai + bi; wmr[i >> 2][i & 3] = ar - br; wmi[i >> 2][i & 3] = ai - bi;
      }
#pragma unroll
      for (int tp = 0; tp < 2; ++tp) {
        f32x4* wq = (f32x4*)(sm + L_WST + w * 8192) + l;
        wq[(4 * tp) * 64] = wpr[tp]; wq[(4 * tp + 1) * 64] = wpi[tp]; wq[(4 * tp + 2) * 64] = wmr[tp]; wq[(4 * tp + 3) * 64] = wmi[tp];
      }
    }
    STAMP(6);
    BAR();
    STAMP(7);
    const unsigned wown = OFF_WGU + min(ft, 85) * 32768, wpar = OFF_WGU + min(ft - 4, 85) * 32768;
    auto loadW = [&](int ks) __attribute__((always_inline)) {
      wf[ks][0] = ld16(R, l * 16 + (ks & 1) * 2048, wown + (ks >> 1) * 4096);
      wf[ks][1] = ld16(R, l * 16 + (ks & 1) * 2048 + 1024, wown + (ks >> 1) * 4096);
    };
    u32x4* wst = (u32x4*)(sm + L_WST + w * 32768) + l;
    u32x4 pw[16];
#pragma unroll
    for (int i = 0; i < 16; ++i) pw[i] = ld16(R, l * 16 + (i & 3) * 1024, wpar + (i >> 2) * 4096);
#pragma unroll
    for (int ks = 0; ks < HW0; ++ks) loadW(ks);
    STAMP(8);
    if (b == 0) BAR();
    BAR();
#pragma unroll
    for (int i = 0; i < 16; ++i) wst[i * 64] = pw[i];
    FENCE();
#pragma unroll
    for (int i = 0; i < 16; ++i) pw[i] = ld16(R, l * 16 + (i & 3) * 1024, wpar + 16384 + (i >> 2) * 4096);
#pragma unroll
    for (int ks = HW0; ks < HW1; ++ks) loadW(ks);
    FENCE();
#pragma unroll
    for (int i = 0; i < 16; ++i) wst[(16 + i) * 64] = pw[i];
    STAMP(9);
    BAR();
#pragma unroll
    for (int ks = HW1; ks < 16; ++ks) loadW(ks);
    STAMP(10);
    BAR();
    st16(*(const u32x4*)(sm + L_X1 + t4 * 16), R, t4 * 16, OFF_X1 + b * 8192);
    st16(*(const u32x4*)(sm + L_X1 + 4096 + t4 * 16), R, t4 * 16 + 4096, OFF_X1 + b * 8192);
  }
  FENCE();
  STAMP(11);

  const int tg = xcd * 4 + (loc >> 3), dg = loc & 7;
  u32x4 wdf[6][4];
  auto loadD = [&](int i) __attribute__((always_inline)) {
    const unsigned base = OFF_WD + (dg * 43 + min(8 * i + w8, 42)) * 4096;
#pragma unroll
    for (int j = 0; j < 4; ++j) wdf[i][j] = ld16(R, l * 16 + j * 1024, base);
  };
  if (g1_on) {
    BAR();
    STAMP(18);
    u32x4* As = (u32x4*)(sm + L_AS) + ((w8 >> 1) * 6 + 3 * (w8 & 1)) * 64 + l;
    const u32x4* Ar = (const u32x4*)(sm + L_AS) + l;
    u32x4 ar[4][3];
    unsigned abase[3];
#pragma unroll
    for (int i = 0; i < 3; ++i) abase[i] = OFF_H2 + min(6 * pm + 3 * (w8 & 1) + i, 127) * 16384 + (w8 >> 1) * 1024;
    auto loadA = [&](int qd) __attribute__((always_inline)) {
#pragma unroll
      for (int i = 0; i < 3; ++i) ar[qd][i] = ld16(R, l * 16, abase[i] + qd * 4096);
    };
    auto badA = [&](int qd) __attribute__((always_inline)) {
      return wave_any(vmax(ar[qd][0], vmax(ar[qd][1], vmax(ar[qd][2], 0u))) == SENT);
    };
    auto stageA = [&](int qd) __attribute__((always_inline)) {
      if (__builtin_expect(badA(qd), 0)) {
        unsigned spins = 0;
        do { __builtin_amdgcn_s_sleep(2); FENCE(); loadA(qd); } while (badA(qd) && ++spins < SPIN_CAP);
      }
#pragma unroll
      for (int i = 0; i < 3; ++i) As[(24 * qd + i) * 64] = ar[qd][i];
    };
    loadA(0); loadA(1);
    FENCE();
    f32x4 acc[2][6];
#pragma unroll
    for (int f = 0; f < 2; ++f)
#pragma unroll
      for (int tt = 0; tt < 6; ++tt) acc[f][tt] = z4;
    stageA(0);
    if (!crit && l == 0) st4(tag, R, 0, OFF_FX + b * 16 + w * 4);
    BAR();
    STAMP(12);
    u32x4 bq[96];
#pragma unroll
    for (int n = 0; n < PF; ++n) bq[n] = Ar[n * 64];
#pragma unroll
    for (int ks = 0; ks < 16; ++ks) {
      SB();
      if (ks == 0) loadA(2);
      if (ks == 3) loadA(3);
      if (ks == 3) loadD(0);
      if (ks == 5) loadD(1);
      if (ks >= 7 && ks < 11) loadD(ks - 5);
      SB();
#pragma unroll
      for (int tt = 0; tt < 6; ++tt) {
        const int n = ks * 6 + tt;
        if (n % 24 == 24 - PF - 4 && n < 72) { stageA(n / 24 + 1); }
        if (n % 24 == 24 - PF && n < 72) { BAR(); }
        if (n + PF < 96) bq[n + PF] = Ar[(n + PF) * 64];
        acc[0][tt] = mfma(wf[ks][0], bq[n], acc[0][tt]);
        acc[1][tt] = mfma(wf[ks][1], bq[n], acc[1][tt]);
        SB();
      }
    }
    STAMP(13);
    if (ft < 86) {
#pragma unroll
      for (int pr = 0; pr < 3; ++pr) {
        u32x4 o;
#pragma unroll
        for (int q = 0; q < 2; ++q)
#pragma unroll
          for (int e2 = 0; e2 < 2; ++e2) {
            const unsigned gg = pk(acc[0][2 * pr + q][2 * e2], acc[0][2 * pr + q][2 * e2 + 1]);
            const unsigned uu = pk(acc[1][2 * pr + q][2 * e2], acc[1][2 * pr + q][2 * e2 + 1]);
            o[2 * q + e2] = pk(lo(gg) * lo(uu), hi(gg) * hi(uu));
          }
        if (3 * pm + pr < 64) st16(o, R, l * 16, OFF_GU + ((3 * pm + pr) * 86 + ft) * 1024);
      }
    }
    DRAIN();
    BAR();
  } else {
#pragma unroll
    for (int i = 0; i < 6; ++i) loadD(i);
    DRAIN();
    if (!crit && l == 0) st4(tag, R, 0, OFF_FX + b * 16 + w * 4);
    BAR();
    for (unsigned spins = 0;; ++spins) {
      FENCE();
      const unsigned v = ld4(R, (l & 1) * 86 * 1024, OFF_GU + (2 * tg * 86 + 40) * 1024);
      if (!wave_any(v == SENT) || spins > SPIN_CAP) break;
      __builtin_amdgcn_s_sleep(32);
    }
  }
  FENCE();
  STAMP(14);

  {
    int tz = threadIdx.x;
    PIN(tz);
    const int l = tz & 63, c = l & 15, g = l >> 4;
    const int tt_o = w8 >> 1, tp_o = w8 & 1;
    const int c3 = 2 * (4 * tg + tt_o) + (c >> 3);
    const int row = 32 * (4 * (c3 >> 5) + (c & 3)) + (c3 & 31) + 1024 * ((c >> 2) & 1);
    STAMP(19);
    u32x4 gf[6][2][2];
    auto loadG = [&](int i) __attribute__((always_inline)) {
      const unsigned base = OFF_GU + (2 * tg * 86 + 2 * min(8 * i + w8, 42)) * 1024;
#pragma unroll
      for (int pr = 0; pr < 2; ++pr) {
        gf[i][pr][0] = ld16(R, l * 16, base + pr * 86 * 1024);
        gf[i][pr][1] = ld16(R, l * 16 + 1024, base + pr * 86 * 1024);
      }
    };
    auto badG = [&](int i) __attribute__((always_inline)) {
      return wave_any(vmax(gf[i][0][0], vmax(gf[i][0][1], vmax(gf[i][1][0], vmax(gf[i][1][1], 0u)))) == SENT);
    };
#pragma unroll
    for (int i = 0; i < GWIN; ++i) loadG(i);
    FENCE();
    f32x4 acc[4][4];
#pragma unroll
    for (int j = 0; j < 4; ++j)
#pragma unroll
      for (int tt = 0; tt < 4; ++tt) acc[j][tt] = z4;
    u32x4 gq[2];
    unsigned fx;
    const unsigned fxoff = 2 * (4 * tg + tt_o) * 16 + (l & 7) * 4;
    const unsigned gboff = (32 * dg + 16 * tp_o + 4 * g) * 8;
#pragma unroll
    for (int i = 0; i < 6; ++i) {
      if (__builtin_expect(badG(i), 0)) {
        unsigned spins = 0;
        do { __builtin_amdgcn_s_sleep(2); FENCE(); loadG(i); } while (badG(i) && ++spins < SPIN_CAP);
      }
      if (i < 5 || w8 < 3) {
#pragma unroll
        for (int tt = 0; tt < 4; ++tt) {
          const u32x4 lo4 = gf[i][tt >> 1][0], hi4 = gf[i][tt >> 1][1];
          const u32x4 bf = {lo4[2 * (tt & 1)], lo4[2 * (tt & 1) + 1], hi4[2 * (tt & 1)], hi4[2 * (tt & 1) + 1]};
#pragma unroll
          for (int j = 0; j < 4; ++j) acc[j][tt] = mfma(wdf[i][j], bf, acc[j][tt]);
        }
      }
      FENCE();
      if (i + GWIN < 6) loadG(i + GWIN);
      if (i == 6 - GWIN) {
        fx = ld4(R, fxoff, OFF_FX);
        gq[0] = ld16(R, gboff, OFF_MODT + 6144); gq[1] = ld16(R, gboff + 16, OFF_MODT + 6144);
      }
      FENCE();
    }
    STAMP(15);
    if (__builtin_expect(wave_any(fx != tag), 0)) wait_flags(R, fxoff, OFF_FX, tag, true);
    FENCE();
    const u32x4 xr = ld16(R, c3 * 8192 + (c & 7) * 1024 + (64 * dg + 32 * tp_o + 8 * g) * 2, OFF_X1);
    FENCE();
    f32x4* red = (f32x4*)sm + l;
#pragma unroll
    for (int tt = 0; tt < 4; ++tt)
#pragma unroll
      for (int j = 0; j < 4; ++j) red[((tt * 4 + j) * 8 + w8) * 64] = acc[j][tt];
    if (__builtin_expect(wave_any(gq[0][1] != tag || gq[0][3] != tag || gq[1][1] != tag || gq[1][3] != tag), 0)) {
      unsigned spins = 0;
      do {
        __builtin_amdgcn_s_sleep(4); FENCE();
        gq[0] = ld16(R, gboff, OFF_MODT + 6144); gq[1] = ld16(R, gboff + 16, OFF_MODT + 6144);
      } while (wave_any(gq[0][1] != tag || gq[0][3] != tag || gq[1][1] != tag || gq[1][3] != tag) && ++spins < SPIN_CAP);
    }
    const u32x4 gbv = {gq[0][0], gq[0][2], gq[1][0], gq[1][2]};
    BAR();
    f32x4 mine[2] = {z4, z4};
#pragma unroll
    for (int s = 0; s < 8; ++s)
#pragma unroll
      for (int j = 0; j < 2; ++j) mine[j] += red[((2 * w8 + j) * 8 + s) * 64];
    u32x4 o;
#pragma unroll
    for (int m = 0; m < 4; ++m) {
      const f32x4 v = mine[m >> 1];
      const unsigned y = pk(v[2 * (m & 1)], v[2 * (m & 1) + 1]);
      const unsigned gy = pk(lo(gbv[m]) * lo(y), hi(gbv[m]) * hi(y));
      o[m] = pk(lo(xr[m]) + lo(gy), hi(xr[m]) + hi(gy));
    }
    *(u32x4*)(out + (size_t)row * D_ + 64 * dg + 32 * tp_o + 8 * g) = o;
  }
  STAMP(16);
  STAMP_FLUSH();
  asm volatile(".Lfb_end:");
}

extern "C" void kernel_launch(void* const* d_in, const int* in_sizes, int n_in,
                              void* d_out, int out_size, void* d_ws, size_t ws_size,
                              hipStream_t stream) {
  (void)hipFuncSetAttribute((const void*)fused_block, hipFuncAttributeMaxDynamicSharedMemorySize, LDS_BYTES);
  fused_block<<<256, 512, LDS_BYTES, stream>>>((const u16*)d_in[0], (const u16*)d_in[1], (const u16*)d_in[2],
                                               (const u16*)d_in[4], (const u16*)d_in[5], (const u16*)d_in[6],
                                               (const u16*)d_in[7], (const u16*)d_in[8], (u16*)d_out, (char*)d_ws);
}
```

```cpp
#include <hip/hip_runtime.h>
#include <hip/hip_bf16.h>
#include <stdint.h>

typedef __attribute__((ext_vector_type(8))) __bf16 bf16x8;
typedef __attribute__((ext_vector_type(2))) __bf16 bf16x2;
typedef __attribute__((ext_vector_type(4))) short s16x4;
typedef __attribute__((ext_vector_type(4))) float f32x4;
typedef __attribute__((ext_vector_type(2))) float f32x2;
typedef __attribute__((ext_vector_type(4))) unsigned u32x4;
typedef __attribute__((ext_vector_type(2))) unsigned u32x2;
typedef unsigned short u16;
typedef __amdgpu_buffer_rsrc_t rsrc_t;

#define DI __device__ __forceinline__
#define LAS __attribute__((address_space(3)))

constexpr int D_ = 512, F_ = 1376, DF = 257;
constexpr unsigned SENT = 0xFFFFFFFFu;
constexpr unsigned SPIN_CAP = 1u << 15;
constexpr unsigned OOB = 0xF0000000u;

constexpr unsigned OFF_MOD  = 0;
constexpr unsigned OFF_GA   = 69632;
constexpr unsigned OFF_G1   = 86016;
constexpr unsigned OFF_EP   = 28672;
constexpr unsigned OFF_GT   = 65536;
constexpr unsigned OFF_FX   = 24576;
constexpr unsigned OFF_FE   = 98304;
constexpr unsigned OFF_WT   = 147456;
constexpr unsigned OFF_WGU  = OFF_WT + 257u * 2048u * 4u;
constexpr unsigned OFF_WD   = OFF_WGU + 86u * 16u * 2u * 1024u;
constexpr unsigned OFF_T1   = OFF_WD + 8u * 43u * 4u * 1024u;
constexpr unsigned OFF_T2   = OFF_T1 + (2u << 20);
constexpr unsigned OFF_H2   = OFF_T2 + (2u << 20);
constexpr unsigned OFF_X1   = OFF_H2 + (2u << 20);
constexpr unsigned OFF_GU   = OFF_X1 + (2u << 20);
constexpr unsigned WS_BYTES = OFF_GU + 64u * 86u * 1024u;

constexpr int L_MODA = 2048, L_IMG1 = 4096, L_INB = 12544, L_EX = 20736;
constexpr int L_MOD3 = 28928, L_CNT = 32000, L_WST = 32768;
constexpr int L_REV = L_IMG1;
constexpr int L_IMG3 = 0, L_H2 = 12544, L_X1 = 20736, L_AS = 32768;
constexpr int LDS_BYTES = 163840;

constexpr int UWT = 6;
constexpr int HG0 = 16;
constexpr int HW0 = 4;
constexpr int PF = 4;
constexpr int FW = 14;

DI unsigned pk(float a, float b) {
  f32x2 v = {a, b};
  return __builtin_bit_cast(unsigned, __builtin_convertvector(v, bf16x2));
}
DI float lo(unsigned u) { return __uint_as_float(u << 16); }
DI float hi(unsigned u) { return __uint_as_float(u & 0xffff0000u); }
DI float rb(float a) { return lo(pk(a, a)); }
DI f32x4 mfma(u32x4 a, u32x4 b, f32x4 c) {
  return __builtin_amdgcn_mfma_f32_16x16x32_bf16(__builtin_bit_cast(bf16x8, a), __builtin_bit_cast(bf16x8, b), c, 0, 0, 0);
}
DI void mfma_acc(f32x4& d, u32x4 a, u32x4 b) { asm volatile("v_mfma_f32_16x16x32_bf16 %0, %1, %2, %0" : "+v"(d) : "v"(a), "v"(b)); }
DI void mfma_new(f32x4& d, u32x4 a, u32x4 b) { asm volatile("v_mfma_f32_16x16x32_bf16 %0, %1, %2, 0" : "=&v"(d) : "v"(a), "v"(b)); }
DI float cosr(float rev) { return __builtin_amdgcn_cosf(rev); }
DI float sinr(float rev) { return __builtin_amdgcn_sinf(rev); }

DI u32x4 ld16(rsrc_t r, unsigned v, unsigned s) { return __builtin_amdgcn_raw_buffer_load_b128(r, v, s, 16); }
DI unsigned ld4(rsrc_t r, unsigned v, unsigned s) { return __builtin_amdgcn_raw_buffer_load_b32(r, v, s, 16); }
DI void st16(u32x4 x, rsrc_t r, unsigned v, unsigned s) { __builtin_amdgcn_raw_buffer_store_b128(x, r, v + s, 0, 16); }
DI void st4(unsigned x, rsrc_t r, unsigned v, unsigned s) { __builtin_amdgcn_raw_buffer_store_b32(x, r, v, s, 16); }

#define FENCE() do { asm volatile("" ::: "memory"); __builtin_amdgcn_sched_barrier(0); } while (0)
#define BAR() do { asm volatile("s_waitcnt lgkmcnt(0)" ::: "memory"); __builtin_amdgcn_s_barrier(); FENCE(); } while (0)
#define DRAIN() asm volatile("s_waitcnt vmcnt(0)" ::: "memory")
#define SB() __builtin_amdgcn_sched_barrier(0)
#define PIN(x) asm volatile("" : "+v"(x))

DI void glds16(const void* src, unsigned lds_base) {
  unsigned keep;
  asm volatile("s_mov_b32 %0, m0\n\ts_mov_b32 m0, %2\n\ts_nop 0\n\tglobal_load_lds_dwordx4 %1, off\n\ts_mov_b32 m0, %0"
               : "=&s"(keep) : "v"(src), "s"(lds_base) : "memory");
}
DI unsigned umax3(unsigned a, unsigned b, unsigned c) { return max(max(a, b), c); }
DI unsigned vmax(u32x4 v, unsigned m) { return umax3(v[2], v[3], umax3(v[0], v[1], m)); }
DI bool wave_any(bool p) { return __builtin_amdgcn_ballot_w64(p) != 0ull; }

template <int CTRL> DI float dpp_add(float v) {
  int s = __builtin_amdgcn_update_dpp(0, __float_as_int(v), CTRL, 0xf, 0xf, true);
  return v + __int_as_float(s);
}
DI float row_sum(float v) { v = dpp_add<0xB1>(v); v = dpp_add<0x4E>(v); v = dpp_add<0x141>(v); return dpp_add<0x140>(v); }
DI float wave_sum(float v) {
  const int i = __float_as_int(row_sum(v));
  const float a = __int_as_float(__builtin_amdgcn_readlane(i, 0)), b = __int_as_float(__builtin_amdgcn_readlane(i, 16));
  const float c = __int_as_float(__builtin_amdgcn_readlane(i, 32)), d = __int_as_float(__builtin_amdgcn_readlane(i, 48));
  return (a + b) + (c + d);
}
DI void ln_stats2(const float (&x)[8], const float (&y)[8], float& mx, float& ix, float& my, float& iy) {
  const float px = __int_as_float(__builtin_amdgcn_readfirstlane(__float_as_int(x[0])));
  const float py = __int_as_float(__builtin_amdgcn_readfirstlane(__float_as_int(y[0])));
  float sx = 0.f, qx = 0.f, sy = 0.f, qy = 0.f;
#pragma unroll
  for (int i = 0; i < 8; ++i) {
    const float d = x[i] - px, e = y[i] - py;
    sx += d; qx += d * d; sy += e; qy += e * e;
  }
  sx = wave_sum(sx) * (1.f / 512.f); qx = wave_sum(qx) * (1.f / 512.f);
  sy = wave_sum(sy) * (1.f / 512.f); qy = wave_sum(qy) * (1.f / 512.f);
  const unsigned var = pk(fmaxf(qx - sx * sx, 0.f), fmaxf(qy - sy * sy, 0.f));
  const unsigned ve = pk(lo(var) + 1e-5f, hi(var) + 1e-5f);
  const unsigned sd = pk(__builtin_amdgcn_sqrtf(lo(ve)), __builtin_amdgcn_sqrtf(hi(ve)));
  const unsigned mean = pk(px + sx, py + sy);
  mx = lo(mean); my = hi(mean);
  ix = __builtin_amdgcn_rcpf(lo(sd)); iy = __builtin_amdgcn_rcpf(hi(sd));
}
DI float silu_b(unsigned bits) {
  const float c = lo(bits);
  return rb(c * __builtin_amdgcn_rcpf(1.f + __expf(-c)));
}
DI u32x4 twiddle(f32x4 re, f32x4 im, f32x4 C, f32x4 S) {
  const f32x4 r = re * C + im * S, i = im * C - re * S;
  return (u32x4){pk(r[0], i[0]), pk(r[1], i[1]), pk(r[2], i[2]), pk(r[3], i[3])};
}
DI void dft_frag(float base, float step, u32x4& fre, u32x4& fim) {
#pragma unroll
  for (int e = 0; e < 4; ++e) {
    const float a = base + (float)e * step, cs = cosr(a), sn = sinr(a);
    fre[e] = pk(cs, sn); fim[e] = pk(-sn, cs);
  }
}
DI void tw_frag(float base, float step, f32x4& C, f32x4& S) {
#pragma unroll
  for (int e = 0; e < 4; ++e) { const float a = base + (float)e * step; C[e] = cosr(a); S[e] = sinr(a); }
}
DI u32x4 flip(u32x4 v, unsigned m) { return (u32x4){v[0] ^ m, v[1] ^ m, v[2] ^ m, v[3] ^ m}; }
DI u32x4 flip_odd(u32x4 v) { return (u32x4){v[0], v[1] ^ 0x80008000u, v[2], v[3] ^ 0x80008000u}; }
DI unsigned lds_add(char* p) { return __hip_atomic_fetch_add((LAS unsigned*)p, 1u, __ATOMIC_RELAXED, __HIP_MEMORY_SCOPE_WORKGROUP); }
DI unsigned lds_get(char* p) { return __hip_atomic_load((LAS unsigned*)p, __ATOMIC_RELAXED, __HIP_MEMORY_SCOPE_WORKGROUP); }
DI void lds_wait(char* p, unsigned want) {
  for (unsigned spins = 0; spins < (1u << 20); ++spins) {
    if (__builtin_amdgcn_readfirstlane(__hip_atomic_load((LAS unsigned*)p, __ATOMIC_RELAXED, __HIP_MEMORY_SCOPE_WORKGROUP)) >= want) break;
    __builtin_amdgcn_s_sleep(1);
  }
  FENCE();
}
DI u32x2 tr_read(const char* p) {
  return __builtin_bit_cast(u32x2, __builtin_amdgcn_ds_read_tr16_b64_v4i16((LAS s16x4*)p));
}

DI void wait_flags(rsrc_t R, unsigned voff, unsigned soff, unsigned tag, bool active) {
  for (unsigned spins = 0;; ++spins) {
    FENCE();
    const unsigned v = ld4(R, voff, soff);
    if (!wave_any(active && v != tag) || spins > SPIN_CAP) break;
    __builtin_amdgcn_s_sleep(2);
  }
  FENCE();
}

__global__ __launch_bounds__(512, 2) void fused_block(const u16* __restrict__ x, const u16* __restrict__ wr,
                                                      const u16* __restrict__ wi, const u16* __restrict__ wg,
                                                      const u16* __restrict__ wu, const u16* __restrict__ wd,
                                                      const u16* __restrict__ cvec, const u16* __restrict__ wmod,
                                                      u16* out, char* ws) {
  extern __shared__ __attribute__((aligned(16))) char sm[];
  const int b = blockIdx.x, tid = threadIdx.x, w8 = __builtin_amdgcn_readfirstlane(tid >> 6), l = tid & 63, c = l & 15, g = l >> 4;
  const int w = w8 & 3, t4 = tid & 255;
  const bool crit = w8 < 4;
  const rsrc_t R = __builtin_amdgcn_make_buffer_rsrc(ws, 0, WS_BYTES, 0x00020000);
  const rsrc_t RM = __builtin_amdgcn_make_buffer_rsrc((void*)wmod, 0, 512 * 3072 * 2, 0x00020000);
  const f32x4 z4 = {0.f, 0.f, 0.f, 0.f};
  const unsigned lds0 = (unsigned)(size_t)(LAS char*)sm;
  asm volatile(".Lfb_begin:");
  asm volatile("" :: "s"(x), "s"(wr), "s"(wi), "s"(wg), "s"(wu), "s"(wd), "s"(cvec), "s"(wmod), "s"(out), "s"(ws));
  if (tid < 16) ((unsigned*)(sm + L_CNT))[tid] = 0u;

  const int xcd = b & 7, loc = b >> 3;
  const bool g1_on = loc < 30 + (xcd < 2);
  const int T1i = g1_on ? xcd * 30 + (xcd < 2 ? xcd : 2) + loc : 0;
  const int hh = T1i >= 121, T1j = T1i - 121 * hh, pn = T1j / 11, pm = 11 * hh + (T1j - 11 * pn);
  const int ft = 8 * pn + w8;
  u32x4 wf[16][2];
  const int pb = (b & 7) * 32 + (b >> 3);
  const int tg = xcd * 4 + (loc >> 3), dg = loc & 7;

  const unsigned ep_v = ld4(R, 0, OFF_EP);
  if (w8 >= 6) {
    unsigned long long pc; unsigned ofs, len;
    asm volatile("s_getpc_b64 %0\n.Lfb_here:\n\ts_mov_b32 %1, .Lfb_here-.Lfb_begin\n\ts_mov_b32 %2, .Lfb_end-.Lfb_begin" : "=s"(pc), "=s"(ofs), "=s"(len));
    const char* code = (const char*)((pc - ofs) & ~15ull);
    glds16(code + min((unsigned)((w8 - 6) * 32768 + loc * 1024 + l * 16), (len - 16u) & ~15u), lds0 + L_INB);
  }
  const bool mv_on = b < 192;
  const int slice = ((b >> 3) >> 3) * 64 + (b & 7) * 8 + ((b >> 3) & 7), jg = b - 192;
  u32x4 wm[2];
  unsigned cv[2];
  if (mv_on) {
#pragma unroll
    for (int i = 0; i < 2; ++i) {
      const int r0 = (slice & 1) * 256 + 128 * i;
      wm[i] = __builtin_amdgcn_raw_buffer_load_b128(RM, (tid >> 2) * 6144 + (l & 3) * 16, (slice >> 1) * 64 + r0 * 6144, 0);
      cv[i] = cvec[(tid >> 2) + r0];
    }
  } else {
    if (w8 == 0) glds16((const u32x4*)(((l & 4) ? wi : wr) + (size_t)32 * jg * DF) + 1024 + (l & 3), lds0 + L_WST + 32768);
#pragma unroll
    for (int i = 0; i < 4; ++i) glds16((const u32x4*)(((w8 & 4) ? wi : wr) + (size_t)32 * jg * DF) + ((w8 & 3) * 4 + i) * 64 + l, lds0 + L_WST + (4 * w8 + i) * 1024);
  }
  const int gw = mv_on ? b * 8 + w8 : 1536 + jg * UWT + w8, gw2 = 1536 + 64 * UWT + (b - 8) + (7 - w8) * 184;
  const int nu = mv_on ? ((b >= 8 && w8 >= 6 && gw2 < 2064) ? 2 : 1) : (w8 < UWT ? 1 : 0);
  const unsigned hs = L_WST + 36864 + w8 * 4096;
  for (int i = 0; i < nu; ++i) {
    const int u = i ? gw2 : gw;
    const u16* src; int pitch;
    if (u < 1376) {
      const int mat = u >= 688, v = u - 688 * mat, ks = v / 43, wc = v - 43 * ks;
      src = (mat ? wu : wg) + (size_t)(32 * ks) * F_ + 32 * wc; pitch = F_;
    } else {
      const int v = u - 1376;
      src = wd + (size_t)(32 * (v >> 4)) * D_ + 32 * (v & 15); pitch = D_;
    }
    src += (size_t)(l >> 2) * pitch + 8 * (l & 3);
    glds16(src, lds0 + hs + i * 2048);
    glds16(src + (size_t)16 * pitch, lds0 + hs + i * 2048 + 1024);
  }
  const u32x4 SENT4 = {SENT, SENT, SENT, SENT};
  FENCE();
  st16(SENT4, R, tid * 16, OFF_T1 + b * 8192);
  FENCE();
  u32x4 k16[2], k32a[2][2][2], k32[2][2][2], k1c[4][2], k4c[4][2];
  f32x4 tw1c[2], tw1s[2], t1c[2], t1s[2], t3c[2], t3s[2];
  float na[8], nb[8];
  unsigned ua[4], ub[4];
  const float fc = (float)c, fg4 = (float)(4 * g), fj1 = (float)(16 * w + c);
  if (crit) {
    const int sp1 = 32 * (8 * ((b >> 5) & 3) + 2 * ((b >> 3) & 3) + (w >> 1)) + 16 * (b >> 7) + 2 * (b & 7) + (w & 1);
    const unsigned* xa = (const unsigned*)(x + (size_t)sp1 * D_) + 64 * g + c;
#pragma unroll
    for (int e = 0; e < 4; ++e) { ua[e] = xa[16 * e]; ub[e] = xa[16 * e + 1024 * D_ / 2]; }
    FENCE();
    const unsigned codd = (c & 1) ? 0x80008000u : 0u;
    dft_frag(fg4 * fc * (1.f / 16.f), fc * (1.f / 16.f), k16[0], k16[1]);
    dft_frag((2.f * fg4 + 1.f) * fc * (1.f / 32.f), fc * (1.f / 16.f), k32a[0][0][1], k32a[0][1][1]);
#pragma unroll
    for (int pt = 0; pt < 2; ++pt) {
      k32a[1][pt][1] = flip(k32a[0][pt][1], 0x80008000u);
      PIN(k32a[0][pt][1]); PIN(k32a[1][pt][1]);
    }
#pragma unroll
    for (int t = 0; t < 2; ++t) {
      tw_frag((2.f * fg4 + t) * fc * (1.f / 512.f), fc * (1.f / 256.f), tw1c[t], tw1s[t]);
      PIN(tw1c[t]); PIN(tw1s[t]);
    }
    PIN(k16[0]); PIN(k16[1]);
    dft_frag(fg4 * fc * (1.f / 32.f), fc * (1.f / 32.f), k32[0][0][0], k32[0][1][0]);
#pragma unroll
    for (int pt = 0; pt < 2; ++pt) {
      k32[0][pt][1] = flip(k32[0][pt][0], codd);
      k32[1][pt][0] = flip_odd(k32[0][pt][0]);
      k32[1][pt][1] = flip_odd(k32[0][pt][1]);
      PIN(k32[0][pt][0]); PIN(k32[0][pt][1]); PIN(k32[1][pt][0]); PIN(k32[1][pt][1]);
    }
    FENCE();
#pragma unroll
    for (int t = 0; t < 2; ++t) {
      tw_frag((fg4 + 16.f * t) * fj1 * (1.f / 2048.f), fj1 * (1.f / 2048.f), t1c[t], t1s[t]);
      PIN(t1c[t]); PIN(t1s[t]);
    }
    {
      const float sg = (c & 1) ? -1.f : 1.f;
      float gr[8], gi[8];
#pragma unroll
      for (int i = 0; i < 8; ++i) {
        const float a = (0.5f * fg4 + (float)(8 * (i >> 1) + (i & 1))) * (fj1 * (1.f / 64.f));
        gr[i] = cosr(a); gi[i] = -sinr(a);
      }
      if (b != 0) {
#pragma unroll
        for (int i = 0; i < 8; ++i) {
          const float A = 0.5f * (gr[i] + sg * gi[i]), B = 0.5f * (gr[i] - sg * gi[i]);
          k1c[i >> 1][0][i & 1] = pk(A, sg * B); k1c[i >> 1][0][2 + (i & 1)] = pk(B, sg * A);
          k1c[i >> 1][1][i & 1] = pk(-sg * B, A); k1c[i >> 1][1][2 + (i & 1)] = pk(sg * A, -B);
        }
      } else {
#pragma unroll
        for (int i = 0; i < 8; ++i) {
          k1c[i >> 1][0][i & 1] = pk(gr[i], sg * gr[i]); k1c[i >> 1][0][2 + (i & 1)] = pk(-gi[i], -sg * gi[i]);
          k1c[i >> 1][1][i & 1] = pk(gi[i], sg * gi[i]); k1c[i >> 1][1][2 + (i & 1)] = pk(gr[i], sg * gr[i]);
        }
      }
#pragma unroll
      for (int i = 0; i < 8; ++i) PIN(k1c[i >> 1][i & 1]);
    }
    {
#pragma unroll
      for (int t = 0; t < 2; ++t) {
        const float v = (fc + 16.f * t) * (1.f / 2048.f);
        tw_frag(((float)(16 * w) + fg4) * v, v, t3c[t], t3s[t]);
        PIN(t3c[t]); PIN(t3s[t]);
      }
      const int n1p = 8 * w + (c & 7);
      const bool col1 = c >= 8;
      float gr[8], gi[8];
#pragma unroll
      for (int i = 0; i < 8; ++i) {
        const float a = (fg4 + (float)(16 * (i >> 2) + (i & 3))) * ((float)n1p * (1.f / 64.f));
        gr[i] = cosr(a); gi[i] = -sinr(a);
      }
      const float cs1 = col1 ? -1.f : 1.f;
      if (b != 0) {
#pragma unroll
        for (int i = 0; i < 8; ++i) {
          const float s = (i & 1) ? -cs1 : cs1;
          const float U = gr[i] + s * gi[i], V = gr[i] - s * gi[i];
          k4c[i >> 2][0][i & 3] = pk(U, s * V);
          k4c[i >> 2][1][i & 3] = pk(-cs1 * s * V, cs1 * U);
        }
      } else {
#pragma unroll
        for (int i = 0; i < 8; ++i) {
          const float s = (i & 1) ? -1.f : 1.f;
          const float U = col1 ? -gi[i] : gr[i], V = col1 ? -gr[i] : -gi[i];
          k4c[i >> 2][0][i & 3] = pk(U, V);
          k4c[i >> 2][1][i & 3] = pk(-s * U, -s * V);
        }
      }
#pragma unroll
      for (int i = 0; i < 4; ++i) k4c[2 + (i >> 1)][i & 1] = flip(k4c[i >> 1][i & 1], codd);
#pragma unroll
      for (int i = 0; i < 8; ++i) PIN(k4c[i >> 1][i & 1]);
    }
  }
  DRAIN();
  const unsigned tag = __builtin_amdgcn_readfirstlane(ep_v) + 1u;
  if (mv_on) {
    float a[8];
    const float s0 = silu_b(cv[0]), s1 = silu_b(cv[1]);
#pragma unroll
    for (int m = 0; m < 4; ++m) {
      a[2 * m] = s0 * lo(wm[0][m]) + s1 * lo(wm[1][m]);
      a[2 * m + 1] = s0 * hi(wm[0][m]) + s1 * hi(wm[1][m]);
    }
#pragma unroll
    for (int e = 0; e < 8; ++e) a[e] = dpp_add<0x128>(dpp_add<0x124>(a[e]));
    if (c < 4) {
      ((f32x4*)(sm + L_WST + 32768))[(tid >> 4) * 8 + 2 * c] = (f32x4){a[0], a[1], a[2], a[3]};
      ((f32x4*)(sm + L_WST + 32768))[(tid >> 4) * 8 + 2 * c + 1] = (f32x4){a[4], a[5], a[6], a[7]};
    }
    BAR();
    if (w8 < 2) {
      f32x4 s = ((const f32x4*)(sm + L_WST + 32768))[c * 8 + 4 * w8 + g] + ((const f32x4*)(sm + L_WST + 32768))[(16 + c) * 8 + 4 * w8 + g];
#pragma unroll
      for (int e = 0; e < 4; ++e) s[e] = row_sum(s[e]);
      if (slice < 64) {
        if (c < 2) st16((u32x4){__float_as_uint(c ? s[2] : s[0]), tag, __float_as_uint(c ? s[3] : s[1]), tag}, R, (4 * w8 + g) * 32 + c * 16,
                        OFF_GA + (slice & 1) * 8192 + (slice >> 1) * 256);
      } else if (c == 0) {
        st16(__builtin_bit_cast(u32x4, s), R, (4 * w8 + g) * 16, OFF_MOD + (slice & 1) * 12288 + (slice >> 1) * 128);
      }
    }
    if (tid == 128) st4(tag, R, 0, OFF_G1 + b * 16);
  } else {
    BAR();
    if (tid == 128) st4(tag, R, 0, OFF_G1 + b * 16);
    const u16* lr = (const u16*)(sm + L_WST);
    const u16* tr_ = (const u16*)(sm + L_WST + 32768);
    u32x4 o[5];
#pragma unroll
    for (int it = 0; it < 5; ++it) {
      const int P = it < 4 ? (tid >> 3) + 64 * it : 256, h = tid & 7;
#pragma unroll
      for (int e = 0; e < 4; ++e) {
        const int k = (4 * h + e) * DF + P;
        const u16* pr = (it >= 3 && e == 3 && k >= 8192) ? tr_ + (k - 8192) : lr + k;
        const int io = (it >= 3 && e == 3 && k >= 8192) ? 32 : 8192;
        o[it][e] = (unsigned)pr[0] | ((unsigned)pr[io] << 16);
      }
    }
#pragma unroll
    for (int it = 0; it < 4; ++it) st16(o[it], R, ((tid >> 3) + 64 * it) * 8192 + (tid & 7) * 16, OFF_WT + jg * 128);
    if (tid < 8) st16(o[4], R, 256 * 8192 + tid * 16, OFF_WT + jg * 128);
  }
  FENCE();
  st16(SENT4, R, tid * 16, OFF_T2 + b * 8192);
  const int q = c >> 2, p = l & 3;
  for (int i = 0; i < nu; ++i) {
    const int u = i ? gw2 : gw;
    unsigned dst, fst; int base, ts, fs;
    if (u < 1376) {
      const int mat = u >= 688, v = u - 688 * mat, ks = v / 43, wc = v - 43 * ks;
      dst = OFF_WGU + (2 * wc * 16 + ks) * 2048 + mat * 1024; fst = 32768;
      base = (8 * g + q) * 64 + 8 * p; ts = 256; fs = 32;
    } else {
      const int v = u - 1376, ks = v >> 4, dq = v & 15;
      dst = OFF_WD + (((dq >> 1) * 43 + ks) * 4 + 2 * (dq & 1)) * 1024; fst = 1024;
      base = (4 * g + q) * 64 + 16 * p; ts = 1024; fs = 8;
    }
    const char* src = sm + hs + i * 2048 + base;
#pragma unroll
    for (int f = 0; f < 2; ++f) {
      const u32x2 v0 = tr_read(src + f * fs), v1 = tr_read(src + f * fs + ts);
      st16((u32x4){v0[0], v0[1], v1[0], v1[1]}, R, l * 16, dst + f * fst);
    }
  }
  FENCE();
  if (crit) {
    {
#pragma unroll
      for (int e = 0; e < 4; ++e) { na[2 * e] = lo(ua[e]); na[2 * e + 1] = hi(ua[e]); nb[2 * e] = lo(ub[e]); nb[2 * e + 1] = hi(ub[e]); }
      float ma, ia, mb, ib;
      ln_stats2(na, nb, ma, ia, mb, ib);
#pragma unroll
      for (int e = 0; e < 8; ++e) { na[e] = (na[e] - ma) * ia; nb[e] = (nb[e] - mb) * ib; PIN(na[e]); PIN(nb[e]); }
    }
  }
  DRAIN();
  BAR();
  if (tid == 0) st4(tag, R, 0, OFF_GT + b * 16);

  if (crit) {
    __builtin_amdgcn_s_setprio(2);
    {
      u32x4 v[4];
      for (unsigned spins = 0;; ++spins) {
        FENCE();
#pragma unroll
        for (int i = 0; i < 4; ++i) v[i] = ld16(R, t4 * 32 + (i & 1) * 16, OFF_GA + (i >> 1) * 8192);
        bool bad = ld4(R, t4 * 16, OFF_G1) != tag;
#pragma unroll
        for (int i = 0; i < 4; ++i) bad |= v[i][1] != tag || v[i][3] != tag;
        if (!wave_any(bad) || spins > SPIN_CAP) break;
        __builtin_amdgcn_s_sleep(16);
      }
      *(u32x2*)(sm + L_MODA + t4 * 8) = (u32x2){pk(__uint_as_float(v[0][0]) + __uint_as_float(v[2][0]), __uint_as_float(v[0][2]) + __uint_as_float(v[2][2])),
                                                pk(__uint_as_float(v[1][0]) + __uint_as_float(v[3][0]), __uint_as_float(v[1][2]) + __uint_as_float(v[3][2]))};
      BAR();
    }
    if (tid == 0 && b == 0) st4(tag, R, 0, OFF_EP);
    const int sp3 = 32 * (4 * (pb >> 5) + w) + (pb & 31);
    u32x2 xv[2][2], gav[2], sbv[2], cbv[2];
    {
      const unsigned* moda = (const unsigned*)(sm + L_MODA) + 64 * g + c;
      u32x4 af[2];
#pragma unroll
      for (int e = 0; e < 4; ++e) {
        const unsigned us = moda[16 * e], uc = moda[256 + 16 * e];
#pragma unroll
        for (int t = 0; t < 2; ++t) {
          const float ca1 = 1.f + (t ? hi(uc) : lo(uc)), sa = t ? hi(us) : lo(us);
          af[t][e] = pk(na[2 * e + t] * ca1 + sa, nb[2 * e + t] * ca1 + sa);
        }
      }
      u32x4 p1[2];
#pragma unroll
      for (int t = 0; t < 2; ++t) p1[t] = twiddle(mfma(af[t], k16[0], z4), mfma(af[t], k16[1], z4), tw1c[t], tw1s[t]);
      unsigned* img = (unsigned*)(sm + L_IMG1);
      const f32x4 re0 = mfma(k16[0], p1[0], z4), im0 = mfma(k16[1], p1[0], z4);
#pragma unroll
      for (int tp = 0; tp < 2; ++tp) {
        const f32x4 re = mfma(k32a[tp][0][1], p1[1], re0), im = mfma(k32a[tp][1][1], p1[1], im0);
#pragma unroll
        for (int e = 0; e < 4; ++e) {
          const int kk = c + 64 * g + 16 * e;
          const int p = tp ? ((256 - kk) & 255) : kk;
          img[(4 * (w & 1) + 2 * tp + (w >> 1)) * 260 + p] = pk(re[e], im[e]);
        }
      }
      BAR();
#pragma unroll
      for (int rep = 0; rep < 2; ++rep) {
        const int p = (t4 >> 1) + 128 * rep, qq = t4 & 1;
        u32x4 o;
#pragma unroll
        for (int e = 0; e < 4; ++e) o[e] = img[(4 * qq + e) * 260 + p];
        st16(o, R, (p >> 1) * 16384 + (t4 & 3) * 16, OFF_T1 + b * 64);
      }
    }
    FENCE();
#pragma unroll
    for (int tp = 0; tp < 2; ++tp) {
      xv[0][tp] = *(const u32x2*)(x + (size_t)sp3 * D_ + 256 * tp + 16 * c + 4 * g);
      xv[1][tp] = *(const u32x2*)(x + (size_t)(sp3 + 1024) * D_ + 256 * tp + 16 * c + 4 * g);
    }
    FENCE();
    lds_wait(sm + L_CNT + 8, 1u);
    const int j0 = 16 * w + c + 256 * g;
    unsigned wv[8];
#pragma unroll
    for (int i = 0; i < 8; ++i) wv[i] = ld4(R, j0 * 4 + (i & 3) * 256, OFF_WT + pb * 8192 + (i >> 2) * 4096);
    FENCE();
    unsigned dq[8][3];
    if (b == 0) {
#pragma unroll
      for (int i = 0; i < 8; ++i) {
        const int j = j0 + 64 * (16 * (i >> 2) + (i & 3)), jn = (2048 - j) & 2047;
        dq[i][0] = ld4(R, jn * 4, OFF_WT); dq[i][1] = ld4(R, j * 4, OFF_WT + 256 * 8192); dq[i][2] = ld4(R, jn * 4, OFF_WT + 256 * 8192);
      }
    }
    FENCE();
    const float nrm = 1.f / (2048.f * 512.f);
    DRAIN();
    const unsigned inoff = (t4 >> 1) * 64 + (t4 & 1) * 16, inbase = (pb >> 1) * 16384 + (pb & 1) * 32;
    {
      u32x4 v[2];
      for (unsigned spins = 0;; ++spins) {
        FENCE();
        v[0] = ld16(R, inoff, OFF_T1 + inbase);
        v[1] = ld16(R, inoff, OFF_T1 + inbase + 8192);
        if (!wave_any(vmax(v[0], vmax(v[1], 0u)) == SENT) || spins > SPIN_CAP) break;
        __builtin_amdgcn_s_sleep(2);
      }
      ((u32x4*)(sm + L_INB))[t4] = v[0];
      ((u32x4*)(sm + L_INB))[t4 + 256] = v[1];
    }
    BAR();

    {
      const u32x4* inb = (const u32x4*)(sm + L_INB) + l;
      u32x4 p1[2], p2[2];
#pragma unroll
      for (int t = 0; t < 2; ++t) {
        f32x4 re = z4, im = z4;
#pragma unroll
        for (int ks = 0; ks < 4; ++ks) {
          const u32x4 d = inb[(t * 4 + ks) * 64];
          re = mfma(d, k1c[ks][0], re); im = mfma(d, k1c[ks][1], im);
        }
        p1[t] = twiddle(re, im, t1c[t], t1s[t]);
      }
      f32x4 xr[2], xi[2];
#pragma unroll
      for (int tp = 0; tp < 2; ++tp) {
        xr[tp] = mfma(k32[tp][0][0], p1[0], z4); xi[tp] = mfma(k32[tp][1][0], p1[0], z4);
        xr[tp] = mfma(k32[tp][0][1], p1[1], xr[tp]); xi[tp] = mfma(k32[tp][1][1], p1[1], xi[tp]);
      }
      if (b != 0) {
#pragma unroll
        for (int tp = 0; tp < 2; ++tp) {
          f32x4 a, bb;
#pragma unroll
          for (int e = 0; e < 4; ++e) { a[e] = lo(wv[4 * tp + e]); bb[e] = hi(wv[4 * tp + e]); }
          a *= nrm; bb *= -nrm;
          const f32x4 yr = xr[tp] * a + xi[tp] * bb, yi = xr[tp] * bb - xi[tp] * a;
#pragma unroll
          for (int e = 0; e < 4; ++e) p2[tp][e] = pk(yr[e], yi[e]);
        }
      } else {
        unsigned* rev = (unsigned*)(sm + L_REV);
#pragma unroll
        for (int tp = 0; tp < 2; ++tp)
#pragma unroll
          for (int e = 0; e < 4; ++e) rev[j0 + 64 * (16 * tp + e)] = pk(xr[tp][e], xi[tp][e]);
        f32x4 wpr[2], wpi[2], wmr[2], wmi[2];
#pragma unroll
        for (int i = 0; i < 8; ++i) {
          const float sc4 = 0.25f * nrm;
          const float ar = (lo(wv[i]) + lo(dq[i][0])) * sc4, ai = (hi(wv[i]) - hi(dq[i][0])) * sc4;
          const float br = (lo(dq[i][1]) + lo(dq[i][2])) * sc4, bi = (hi(dq[i][1]) - hi(dq[i][2])) * sc4;
          wpr[i >> 2][i & 3] = ar + br; wpi[i >> 2][i & 3] = ai + bi; wmr[i >> 2][i & 3] = ar - br; wmi[i >> 2][i & 3] = ai - bi;
        }
#pragma unroll
        for (int tp = 0; tp < 2; ++tp) { PIN(wpr[tp]); PIN(wpi[tp]); PIN(wmr[tp]); PIN(wmi[tp]); }
        BAR();
#pragma unroll
        for (int tp = 0; tp < 2; ++tp) {
          f32x4 nr, ni;
#pragma unroll
          for (int e = 0; e < 4; ++e) { const unsigned n = rev[(2048 - (j0 + 64 * (16 * tp + e))) & 2047]; nr[e] = lo(n); ni[e] = hi(n); }
          const f32x4 yr = xr[tp] * wpr[tp] - xi[tp] * wpi[tp] + nr * wmr[tp] + ni * wmi[tp];
          const f32x4 yi = ni * wmr[tp] - nr * wmi[tp] - xr[tp] * wpi[tp] - xi[tp] * wpr[tp];
#pragma unroll
          for (int e = 0; e < 4; ++e) p2[tp][e] = pk(yr[e], yi[e]);
        }
      }
      u32x4* ex = (u32x4*)(sm + L_EX);
#pragma unroll
      for (int t = 0; t < 2; ++t) {
        f32x4 re = mfma(p2[0], k32[t][0][0], z4), im = mfma(p2[0], k32[t][1][0], z4);
        re = mfma(p2[1], k32[t][0][1], re); im = mfma(p2[1], k32[t][1][1], im);
        ex[(w * 2 + t) * 64 + l] = twiddle(re, im, t3c[t], t3s[t]);
      }
      BAR();
#pragma unroll
      for (int t = 0; t < 2; ++t) {
        f32x4 re = z4, im = z4;
#pragma unroll
        for (int ks = 0; ks < 4; ++ks) {
          const u32x4 d = ex[(ks * 2 + t) * 64 + l];
          re = mfma(k4c[ks][0], d, re); im = mfma(k4c[ks][1], d, im);
        }
        u32x4 o;
#pragma unroll
        for (int e = 0; e < 4; ++e) o[e] = pk(re[e], im[e]);
        st16(o, R, ((2 * w + (g & 1)) * 16 + 8 * t + (c >> 1)) * 16384 + (c & 1) * 32 + (g >> 1) * 16, OFF_T2 + pb * 64);
      }
    }
    FENCE();
    f32x4 gaf[2], sbf[2], cbf[2], xaf[2], xbf[2];
#pragma unroll
    for (int tp = 0; tp < 2; ++tp) {
      const char* mv = sm + L_MOD3 + (256 * tp + 16 * c + 4 * g) * 2;
      gav[tp] = *(const u32x2*)mv; sbv[tp] = *(const u32x2*)(mv + 1024); cbv[tp] = *(const u32x2*)(mv + 2048);
      gaf[tp] = (f32x4){lo(gav[tp][0]), hi(gav[tp][0]), lo(gav[tp][1]), hi(gav[tp][1])};
      sbf[tp] = (f32x4){lo(sbv[tp][0]), hi(sbv[tp][0]), lo(sbv[tp][1]), hi(sbv[tp][1])};
      cbf[tp] = (f32x4){1.f + lo(cbv[tp][0]), 1.f + hi(cbv[tp][0]), 1.f + lo(cbv[tp][1]), 1.f + hi(cbv[tp][1])};
      xaf[tp] = (f32x4){lo(xv[0][tp][0]), hi(xv[0][tp][0]), lo(xv[0][tp][1]), hi(xv[0][tp][1])};
      xbf[tp] = (f32x4){lo(xv[1][tp][0]), hi(xv[1][tp][0]), lo(xv[1][tp][1]), hi(xv[1][tp][1])};
      PIN(gaf[tp]); PIN(sbf[tp]); PIN(cbf[tp]); PIN(xaf[tp]); PIN(xbf[tp]);
    }
    f32x4 tw3c[2], tw3s[2];
#pragma unroll
    for (int t = 0; t < 2; ++t) {
      tw_frag((fg4 + 16.f * t) * fc * (1.f / 512.f), fc * (1.f / 512.f), tw3c[t], tw3s[t]);
      PIN(tw3c[t]); PIN(tw3s[t]);
    }
    DRAIN();
    {
      u32x4 v[2];
      for (unsigned spins = 0;; ++spins) {
        FENCE();
        v[0] = ld16(R, inoff, OFF_T2 + inbase);
        v[1] = ld16(R, inoff, OFF_T2 + inbase + 8192);
        if (!wave_any(vmax(v[0], vmax(v[1], 0u)) == SENT) || spins > SPIN_CAP) break;
        __builtin_amdgcn_s_sleep(2);
      }
      unsigned* img = (unsigned*)(sm + L_IMG3);
#pragma unroll
      for (int rep = 0; rep < 2; ++rep) {
        const int p = (t4 >> 1) + 128 * rep;
        const int k = (t4 & 1) ? (p ? 512 - p : 256) : p;
        const int pos = (k >> 5) * 36 + (k & 31);
#pragma unroll
        for (int e = 0; e < 4; ++e) img[e * 580 + pos] = v[rep][e];
      }
    }
    BAR();

    {
      const unsigned* img = (const unsigned*)(sm + L_IMG3) + w * 580 + 4 * g * 36 + c;
      u32x4 p1[2];
#pragma unroll
      for (int t = 0; t < 2; ++t) {
        u32x4 af;
#pragma unroll
        for (int e = 0; e < 4; ++e) af[e] = img[e * 36 + 16 * t];
        p1[t] = twiddle(mfma(af, k16[0], z4), mfma(af, k16[1], z4), tw3c[t], tw3s[t]);
      }
      float x1[2][8];
#pragma unroll
      for (int tp = 0; tp < 2; ++tp) {
        f32x4 re = mfma(p1[0], k32[tp][0][0], z4), im = mfma(p1[0], k32[tp][1][0], z4);
        re = mfma(p1[1], k32[tp][0][1], re); im = mfma(p1[1], k32[tp][1][1], im);
        const f32x4 ya = xaf[tp] + gaf[tp] * re, yb = xbf[tp] - gaf[tp] * im;
#pragma unroll
        for (int e = 0; e < 4; ++e) { x1[0][4 * tp + e] = ya[e]; x1[1][4 * tp + e] = yb[e]; }
      }
      float mean[2], isd[2];
      ln_stats2(x1[0], x1[1], mean[0], isd[0], mean[1], isd[1]);
#pragma unroll
      for (int half = 0; half < 2; ++half) {
        const float m = mean[half], iv = isd[half];
        const int rr = 4 * half + w;
#pragma unroll
        for (int tp = 0; tp < 2; ++tp) {
          const f32x4 v = {x1[half][4 * tp], x1[half][4 * tp + 1], x1[half][4 * tp + 2], x1[half][4 * tp + 3]};
          const f32x4 hv = ((v - m) * iv) * cbf[tp] + sbf[tp];
          const u32x2 xo = {pk(v[0], v[1]), pk(v[2], v[3])}, ho = {pk(hv[0], hv[1]), pk(hv[2], hv[3])};
          *(u32x2*)(sm + L_X1 + rr * 1024 + (256 * tp + 16 * c + 4 * g) * 2) = xo;
          const int kg = 4 * (8 * tp + (c >> 1)) + 2 * (c & 1) + (g >> 1);
          *(u32x2*)(sm + L_H2 + kg * 128 + ((rr + (kg >> 1)) & 7) * 16 + (g & 1) * 8) = ho;
        }
      }
    }
    BAR();
#pragma unroll
    for (int rep = 0; rep < 2; ++rep) {
      const int kg = (t4 >> 3) + 32 * rep, rr = t4 & 7;
      st16(*(const u32x4*)(sm + L_H2 + kg * 128 + ((rr + (kg >> 1)) & 7) * 16), R, kg * 256 + rr * 16, OFF_H2 + (pb >> 1) * 16384 + (pb & 1) * 128);
    }
    FENCE();
    {
      const u32x4* wst = (const u32x4*)(sm + L_WST + w * 32768) + l;
#pragma unroll
      for (int i = 0; i < 32; ++i) wf[i >> 1][i & 1] = wst[i * 64];
#pragma unroll
      for (int i = 0; i < 32; ++i) PIN(wf[i >> 1][i & 1]);
    }
    DRAIN();
  } else {
    BAR();
    BAR();
    if (w == 0) {
      for (unsigned spins = 0;; ++spins) {
        FENCE();
        bool bad = false;
#pragma unroll
        for (int i = 0; i < 4; ++i) bad |= ld4(R, l * 16 + i * 1024, OFF_GT) != tag;
        if (!wave_any(bad) || spins > SPIN_CAP) break;
        __builtin_amdgcn_s_sleep(16);
      }
      if (l == 0) *(volatile unsigned*)(sm + L_CNT + 8) = 1u;
    } else {
      lds_wait(sm + L_CNT + 8, 1u);
    }
    if (t4 < 192) {
      const f32x4 a0 = __builtin_bit_cast(f32x4, ld16(R, t4 * 32, OFF_MOD + 4096)), a1 = __builtin_bit_cast(f32x4, ld16(R, t4 * 32 + 16, OFF_MOD + 4096));
      const f32x4 b0 = __builtin_bit_cast(f32x4, ld16(R, t4 * 32, OFF_MOD + 16384)), b1 = __builtin_bit_cast(f32x4, ld16(R, t4 * 32 + 16, OFF_MOD + 16384));
      const f32x4 s0 = a0 + b0, s1 = a1 + b1;
      *(u32x4*)(sm + L_MOD3 + t4 * 16) = (u32x4){pk(s0[0], s0[1]), pk(s0[2], s0[3]), pk(s1[0], s1[1]), pk(s1[2], s1[3])};
    }
    FENCE();
    const unsigned wown = ft < 86 ? OFF_WGU + ft * 32768 : OOB, wpar = OFF_WGU + (ft - 4) * 32768;
    auto loadW = [&](int ks) __attribute__((always_inline)) {
      wf[ks][0] = ld16(R, l * 16 + (ks & 1) * 2048, wown + (ks >> 1) * 4096);
      wf[ks][1] = ld16(R, l * 16 + (ks & 1) * 2048 + 1024, wown + (ks >> 1) * 4096);
    };
    u32x4* wst = (u32x4*)(sm + L_WST + w * 32768) + l;
    u32x4 pw[32];
#pragma unroll
    for (int i = 0; i < HG0; ++i) pw[i] = ld16(R, l * 16 + (i & 3) * 1024, wpar + (i >> 2) * 4096);
    BAR();
    st16(SENT4, R, t4 * 16, OFF_H2 + b * 8192);
    st16(SENT4, R, t4 * 16 + 4096, OFF_H2 + b * 8192);
    FENCE();
#pragma unroll
    for (int i = HG0; i < 32; ++i) pw[i] = ld16(R, l * 16 + (i & 3) * 1024, wpar + (i >> 2) * 4096);
    FENCE();
    if (b == 0) BAR();
    asm volatile("s_waitcnt vmcnt(%0)" :: "n"(32 - HG0) : "memory");
    BAR();
#pragma unroll
    for (int i = 0; i < 32; ++i) wst[i * 64] = pw[i];
    FENCE();
#pragma unroll
    for (int ks = 0; ks < HW0; ++ks) loadW(ks);
    BAR();
#pragma unroll
    for (int ks = HW0; ks < 16; ++ks) loadW(ks);
    BAR();
    st16(*(const u32x4*)(sm + L_X1 + t4 * 16), R, t4 * 16, OFF_X1 + pb * 8192);
    st16(*(const u32x4*)(sm + L_X1 + 4096 + t4 * 16), R, t4 * 16 + 4096, OFF_X1 + pb * 8192);
  }
  FENCE();

  u32x4 wdf[6][4];
  auto wd_off = [&](int i) __attribute__((always_inline)) { return 8 * i + w8 < 43 ? OFF_WD + (dg * 43 + 8 * i + w8) * 4096 : OOB; };
  auto loadD = [&](u32x4 (&d)[4], int i) __attribute__((always_inline)) {
#pragma unroll
    for (int j = 0; j < 4; ++j) d[j] = ld16(R, l * 16 + j * 1024, wd_off(i));
  };
  u32x4* wdl0 = (u32x4*)(sm + 131072 + w8 * 4096) + l;
  u32x4* wdl1 = (u32x4*)(sm + L_AS + w8 * 4096) + l;
  const int prA = tg < 21 ? 3 * tg : 6 * (tg - 21) + 1, prB = tg < 21 ? 3 * tg + 2 : (tg < 31 ? 6 * (tg - 21) + 4 : 63);
  const unsigned faoff = l < 32 ? OFF_FE + (prA * 11 + min(l, 10)) * 64 : OFF_FX + (4 * ((l & 16) ? prB : prA) + ((l >> 2) & 3)) * 16 + (l & 3) * 4;
  unsigned fa_v = ~tag;
  if (g1_on) {
    BAR();
    u32x4* As = (u32x4*)(sm + L_AS) + w8 * 4 * 64 + l;
    const u32x4* Ar = (const u32x4*)(sm + L_AS) + l;
    u32x4 ar[3][4], tmp[4];
    const unsigned abase = OFF_H2 + w8 * 2048;
    auto loadA = [&](int ps) __attribute__((always_inline)) {
#pragma unroll
      for (int i = 0; i < 4; ++i) ar[ps][i] = ld16(R, l * 16 + (i >> 1) * 1024, 3 * pm + ps < 64 ? abase + (2 * (3 * pm + ps) + (i & 1)) * 16384 : OOB);
    };
    auto badA = [&](int ps) __attribute__((always_inline)) {
      return wave_any(vmax(ar[ps][0], vmax(ar[ps][1], vmax(ar[ps][2], vmax(ar[ps][3], 0u)))) == SENT);
    };
    auto stageA = [&](int ps) __attribute__((always_inline)) {
      if (__builtin_expect(badA(ps), 0)) {
        unsigned spins = 0;
        do { __builtin_amdgcn_s_sleep(2); FENCE(); loadA(ps); } while (badA(ps) && ++spins < SPIN_CAP);
      }
#pragma unroll
      for (int i = 0; i < 4; ++i) As[(32 * ps + i) * 64] = ar[ps][i];
    };
    loadA(0);
    FENCE();
    stageA(0);
    FENCE();
    loadA(1);
    if (!crit && l == 0) st4(tag, R, 0, OFF_FX + pb * 16 + w * 4);
    BAR();
    f32x4 acc[3][2][2];
    u32x4 bq[96], o;
    unsigned arr = 0, seen = 0;
    auto gate = [&](int ps, int k) __attribute__((always_inline)) {
      const int q = k >> 1, e2 = k & 1;
      const unsigned gg = pk(acc[ps][0][q][2 * e2], acc[ps][0][q][2 * e2 + 1]);
      const unsigned uu = pk(acc[ps][1][q][2 * e2], acc[ps][1][q][2 * e2 + 1]);
      o[k] = pk(lo(gg) * lo(uu), hi(gg) * hi(uu));
    };
    auto put = [&](int ps) __attribute__((always_inline)) {
      st16(o, R, (ft < 86 && 3 * pm + ps < 64) ? l * 16 + OFF_GU + ((3 * pm + ps) * 86 + ft) * 1024 : OOB, 0);
    };
#pragma unroll
    for (int n = 0; n < PF; ++n) bq[n] = Ar[n * 64];
    auto pass = [&](int ps) __attribute__((always_inline)) {
#pragma unroll
    for (int m = 0; m < 32; ++m) {
      const int n = 32 * ps + m, ks = m >> 1, tt = m & 1;
      if (m == 0) asm volatile("s_cmp_lt_u32 %0, 4\n\ts_cbranch_scc1 1f\n\ts_setprio %1\n\ts_branch 2f\n1:\n\ts_setprio %2\n2:" :: "s"(w8), "n"(ps != 1), "n"(ps == 1) : "scc");
      if (m == 8 && ps == 0) loadD(wdf[0], 0);
      if (m == 8 && ps == 1) loadD(wdf[1], 1);
      if (m == 8 && ps == 2) loadD(wdf[5], 5);
      if (m == (ps ? 14 : 20)) {
        if (ps < 2) stageA(ps + 1);
        if (ps) {
          u32x4* dst = ps == 1 ? wdl0 : wdl1;
#pragma unroll
          for (int j = 0; j < 4; ++j) dst[j * 64] = tmp[j];
        }
        FENCE();
        if (ps < 2 && l == 0) (void)lds_add(sm + L_CNT + 16 + 4 * ps);
      }
      if (m == FW && ps) {
        asm volatile("s_waitcnt vmcnt(4)" ::: "memory");
        if (l == 0) arr = lds_add(sm + L_CNT + 20 + 4 * ps);
      }
      if (m == FW + 3 && ps && l == 0 && arr == 7u && 3 * pm + ps - 1 < 64) st4(tag, R, 0, OFF_FE + ((3 * pm + ps - 1) * 11 + pn) * 64);
      if (m == 24) {
        if (ps < 2) seen = lds_get(sm + L_CNT + 16 + 4 * ps);
        if (ps == 0) { loadA(2); loadD(tmp, 3); }
        if (ps == 1) { loadD(tmp, 4); }
      }
      if (m == 32 - PF) {
        if (ps < 2 && __builtin_expect(__builtin_amdgcn_readfirstlane(seen) < 8u, 0)) lds_wait(sm + L_CNT + 16 + 4 * ps, 8u);
        if (ps == 1) loadD(wdf[2], 2);
        if (ps == 2) fa_v = ld4(R, faoff, 0);
      }
      SB();
      if (n + PF < 96) bq[n + PF] = Ar[(n + PF) * 64];
      if (ks) mfma_acc(acc[ps][0][tt], wf[ks][0], bq[n]); else mfma_new(acc[ps][0][tt], wf[ks][0], bq[n]);
      if (ps && m >= 2 && m < 6) gate(ps - 1, m - 2);
      if (ks) mfma_acc(acc[ps][1][tt], wf[ks][1], bq[n]); else mfma_new(acc[ps][1][tt], wf[ks][1], bq[n]);
      if (ps && m == 6) put(ps - 1);
      SB();
    }
    };
    pass(0); pass(1); pass(2);
    __builtin_amdgcn_s_setprio(0);
    asm volatile("s_nop 7" : "+v"(acc[2][0][0]), "+v"(acc[2][0][1]), "+v"(acc[2][1][0]), "+v"(acc[2][1][1]));
#pragma unroll
    for (int k = 0; k < 4; ++k) gate(2, k);
    put(2);
    FENCE();
#pragma unroll
    for (int j = 0; j < 4; ++j) { wdf[3][j] = wdl0[j * 64]; wdf[4][j] = wdl1[j * 64]; }
  } else {
#pragma unroll
    for (int i = 0; i < 6; ++i) loadD(wdf[i], i);
    DRAIN();
    if (!crit && l == 0) st4(tag, R, 0, OFF_FX + pb * 16 + w * 4);
  }
  FENCE();

  {
    int tz = threadIdx.x;
    PIN(tz);
    const int l = tz & 63, c = l & 15, g = l >> 4;
    u32x4 gf[2][6][2];
    auto loadG = [&](int h, int pr) __attribute__((always_inline)) {
#pragma unroll
      for (int i = 0; i < 6; ++i) {
        const unsigned base = 8 * i + w8 < 43 ? OFF_GU + (pr * 86 + 2 * (8 * i + w8)) * 1024 : OOB;
        gf[h][i][0] = ld16(R, l * 16, base); gf[h][i][1] = ld16(R, l * 16 + 1024, base);
      }
    };
    if (__builtin_expect(wave_any(fa_v != tag), 0)) wait_flags(R, faoff, 0, tag, true);
    loadG(0, prA);
    FENCE();
    asm volatile("s_waitcnt vmcnt(12)" ::: "memory");
    BAR();
    if (g1_on && pm < 21 && tz == 0) st4(tag, R, 0, OFF_FE + ((3 * pm + 2) * 11 + pn) * 64);
    const int tt_o = w8 >> 1, tp_o = w8 & 1;
    const int c3 = 4 * ((tt_o & 2) ? prB : prA) + 2 * (tt_o & 1) + (c >> 3);
    const int row = 32 * (4 * (c3 >> 5) + (c & 3)) + (c3 & 31) + 1024 * ((c >> 2) & 1);
    const u32x4 xr = ld16(R, c3 * 8192 + (c & 7) * 1024 + (64 * dg + 32 * tp_o + 8 * g) * 2, OFF_X1);
    u32x4 gq[4];
#pragma unroll
    for (int k = 0; k < 4; ++k) gq[k] = ld16(R, (2560 + 64 * dg + 32 * tp_o + 8 * g) * 4 + (k & 1) * 16, OFF_MOD + (k >> 1) * 12288);
    FENCE();
    f32x4* red = (f32x4*)sm + l;
    f32x4 mine[2];
#pragma unroll
    for (int h = 0; h < 2; ++h) {
      f32x4 acc[4][2];
#pragma unroll
      for (int j = 0; j < 4; ++j) { acc[j][0] = z4; acc[j][1] = z4; }
#pragma unroll
      for (int i = 0; i < 6; ++i) {
        if (i < 5 || w8 < 3) {
#pragma unroll
          for (int q = 0; q < 2; ++q) {
            const u32x4 bf = {gf[h][i][0][2 * q], gf[h][i][0][2 * q + 1], gf[h][i][1][2 * q], gf[h][i][1][2 * q + 1]};
#pragma unroll
            for (int j = 0; j < 4; ++j) acc[j][q] = mfma(wdf[i][j], bf, acc[j][q]);
          }
        }
      }
      if (h == 0) {
        FENCE();
        wait_flags(R, (prB * 11 + min(l, 10)) * 64, OFF_FE, tag, true);
        loadG(1, prB);
        FENCE();
      }
#pragma unroll
      for (int t2 = 0; t2 < 4; ++t2) {
        const int ow = 4 * h + t2;
        if (ow != w8) {
          red[((2 * ow) * 8 + w8) * 64] = acc[2 * (t2 & 1)][t2 >> 1];
          red[((2 * ow + 1) * 8 + w8) * 64] = acc[2 * (t2 & 1) + 1][t2 >> 1];
        } else {
          mine[0] = acc[2 * (t2 & 1)][t2 >> 1]; mine[1] = acc[2 * (t2 & 1) + 1][t2 >> 1];
        }
      }
      FENCE();
    }
    const f32x4 gb0 = __builtin_bit_cast(f32x4, gq[0]) + __builtin_bit_cast(f32x4, gq[2]), gb1 = __builtin_bit_cast(f32x4, gq[1]) + __builtin_bit_cast(f32x4, gq[3]);
    const u32x4 gbv = {pk(gb0[0], gb0[1]), pk(gb0[2], gb0[3]), pk(gb1[0], gb1[1]), pk(gb1[2], gb1[3])};
    BAR();
#pragma unroll
    for (int s = 1; s < 8; ++s)
#pragma unroll
      for (int j = 0; j < 2; ++j) mine[j] += red[((2 * w8 + j) * 8 + ((w8 + s) & 7)) * 64];
    u32x4 o;
#pragma unroll
    for (int m = 0; m < 4; ++m) {
      const f32x4 v = mine[m >> 1];
      const unsigned y = pk(v[2 * (m & 1)], v[2 * (m & 1) + 1]);
      const unsigned gy = pk(lo(gbv[m]) * lo(y), hi(gbv[m]) * hi(y));
      o[m] = pk(lo(xr[m]) + lo(gy), hi(xr[m]) + hi(gy));
    }
    const rsrc_t RO = __builtin_amdgcn_make_buffer_rsrc(out, 0, 2048 * 512 * 2, 0x00020000);
    __builtin_amdgcn_raw_buffer_store_b128(o, RO, (row * D_ + 64 * dg + 32 * tp_o + 8 * g) * 2, 0, 16);
  }
  asm volatile(".Lfb_end:");
}

extern "C" void kernel_launch(void* const* d_in, const int* in_sizes, int n_in,
                              void* d_out, int out_size, void* d_ws, size_t ws_size,
                              hipStream_t stream) {
  (void)hipFuncSetAttribute((const void*)fused_block, hipFuncAttributeMaxDynamicSharedMemorySize, LDS_BYTES);
  fused_block<<<256, 512, LDS_BYTES, stream>>>((const u16*)d_in[0], (const u16*)d_in[1], (const u16*)d_in[2],
                                               (const u16*)d_in[4], (const u16*)d_in[5], (const u16*)d_in[6],
                                               (const u16*)d_in[7], (const u16*)d_in[8], (u16*)d_out, (char*)d_ws);
}
```
